```python
import torch

def _softmax(x, axis=-1):
    return torch.softmax(x, dim=axis)
def _gelu(x):
    return torch.nn.functional.gelu(x, approximate="tanh")
def _silu(x):
    return torch.nn.functional.silu(x)
def _sigmoid(x):
    return torch.sigmoid(x)
def _layernorm(x, axis=-1, eps=1e-5):
    m = x.mean(dim=axis, keepdim=True)
    v = x.var(dim=axis, keepdim=True, unbiased=False)
    return (x - m) / torch.sqrt(v + eps)
def _scatter_add(tgt, idx, src, axis=0):
    return tgt.clone().index_add_(axis, idx, src)
def _segment_sum(src, seg, n):
    out = torch.zeros((n,) + src.shape[1:], dtype=src.dtype)
    return out.index_add_(0, seg, src)
def _pad(x, pw):
    flat = tuple(v for pair in reversed(pw) for v in pair)
    return torch.nn.functional.pad(x, flat)

kernel_name = 'interleave_layers__sliding_block__dense_activation_squared_relu__prompt'

def setup_inputs():

    _w = (lambda a: (lambda t: t.to(torch.bfloat16) if t.is_floating_point() else t)(torch.as_tensor(a)))
    def _A_setup():
        B, S, D = 1, 2048, 640
        return {
            "x": _w((lambda *s: torch.randn(*s, dtype=torch.bfloat16) * 0.05)(B, S, D)),
            "wqkv": _w((lambda *s: torch.randn(*s, dtype=torch.bfloat16) * 0.05)(D, 3 * D)),
            "wo": _w((lambda *s: torch.randn(*s, dtype=torch.bfloat16) * 0.05)(D, D)),
        }
    _A_in = _A_setup()

    def _B_setup():
        B, S, D, F = 1, 2048, 640, 1536
        return {
            "x": _w((lambda *s: torch.randn(*s, dtype=torch.bfloat16) * 0.05)(B, S, D)),
            "w1": _w((lambda *s: torch.randn(*s, dtype=torch.bfloat16) * 0.05)(D, F)),
            "b1": _w((lambda *s: torch.randn(*s, dtype=torch.bfloat16) * 0.2)(F)),
            "w2": _w((lambda *s: torch.randn(*s, dtype=torch.bfloat16) * 0.05)(F, D)),
            "b2": _w((lambda *s: torch.randn(*s, dtype=torch.bfloat16) * 0.8)(D)),
        }
    _B_in = _B_setup()

    return {
        "A_x": _A_in["x"],
        "A_wqkv": _A_in["wqkv"],
        "A_wo": _A_in["wo"],
        "B_x": _B_in["x"],
        "B_w1": _B_in["w1"],
        "B_b1": _B_in["b1"],
        "B_w2": _B_in["w2"],
        "B_b2": _B_in["b2"],
    }

def reference(A_x, A_wqkv, A_wo, B_x, B_w1, B_b1, B_w2, B_b2):
    def _A_ref(x, wqkv, wo):
        B, S, D = x.shape
        H = 32; Dh = D // H; W = 256
        qkv = (lambda x, *d: x.permute(*d))((x @ wqkv).reshape(B, S, 3, H, Dh), 2, 0, 3, 1, 4)
        q, k, v = qkv[0], qkv[1], qkv[2]
        scores = torch.einsum("bhqd,bhkd->bhqk", q, k) * (Dh ** -0.5)
        idx = torch.arange(S)
        mask = (idx[:, None] - idx[None, :] >= 0) & (idx[:, None] - idx[None, :] < W)
        scores = torch.where(mask[None, None], scores, -1e9)
        p = _softmax(scores, axis=-1)
        o = torch.einsum("bhqk,bhkd->bhqd", p, v)
        return (lambda x, *d: x.permute(*d))(o, 0, 2, 1, 3).reshape(B, S, D) @ wo

    def _B_ref(x, w1, b1, w2, b2):
        h = torch.einsum("bsd,df->bsf", x, w1) + b1
        zero = torch.zeros_like(h)
        r = torch.where(h > zero, h, zero)
        h = r * r
        out = torch.einsum("bsf,fd->bsd", h, w2) + b2
        return out

    x = A_x
    x = x + _A_ref(_layernorm(x, axis=-1), A_wqkv, A_wo)
    x = x + _B_ref(_layernorm(x, axis=-1), B_w1, B_b1, B_w2, B_b2)
    x = x + _A_ref(_layernorm(x, axis=-1), A_wqkv, A_wo)
    x = x + _B_ref(_layernorm(x, axis=-1), B_w1, B_b1, B_w2, B_b2)
    return x
```

```cpp
#include <hip/hip_runtime.h>
#include <stdint.h>

typedef short bf16x8 __attribute__((ext_vector_type(8)));
typedef short s16x4 __attribute__((ext_vector_type(4)));
typedef float f32x2 __attribute__((ext_vector_type(2)));
typedef float f32x4 __attribute__((ext_vector_type(4)));
typedef float f32x16 __attribute__((ext_vector_type(16)));
typedef unsigned u32x2 __attribute__((ext_vector_type(2)));
typedef unsigned u32x4 __attribute__((ext_vector_type(4)));
typedef __bf16 bf16x2_t __attribute__((ext_vector_type(2)));
typedef unsigned short u16;
#define LAS __attribute__((address_space(3)))
#define DI __device__ __forceinline__

constexpr int S_ = 2048, D_ = 640, F_ = 1536, NQKV = 1920;
constexpr float LN_EPS = 1e-5f;
constexpr float QSCALE = 0.22360679774997896f * 1.4426950408889634f;

DI unsigned cvtpk(float lo, float hi) { f32x2 v = {lo, hi}; bf16x2_t b = __builtin_convertvector(v, bf16x2_t); return __builtin_bit_cast(unsigned, b); }
DI float bflo(unsigned u) { return __uint_as_float(u << 16); }
DI float bfhi(unsigned u) { return __uint_as_float(u & 0xffff0000u); }
DI void dma16(unsigned voff, const void* sbase, unsigned lds) {
  asm volatile("s_mov_b32 m0, %2\n\ts_nop 0\n\tglobal_load_lds_dwordx4 %0, %1" ::"v"(voff), "s"(sbase), "s"(lds) : "memory");
}
template <int N> DI void wait_vm() { asm volatile("s_waitcnt vmcnt(%0)" ::"n"(N) : "memory"); }
DI void bar() { asm volatile("s_barrier" ::: "memory"); }
DI void wait_lgkm_bar() { asm volatile("s_waitcnt lgkmcnt(0)\n\ts_barrier" ::: "memory"); }
DI void sbar() { __builtin_amdgcn_sched_barrier(0); }
DI bf16x8 lds_rd128(unsigned a) { return *(const LAS bf16x8*)a; }
DI s16x4 lds_rd64(unsigned a) { return *(const LAS s16x4*)a; }
DI s16x4 lds_rdtr(unsigned a) { return __builtin_amdgcn_ds_read_tr16_b64_v4i16((LAS s16x4*)a); }
DI bf16x8 cat(s16x4 a, s16x4 b) { return (bf16x8){a[0], a[1], a[2], a[3], b[0], b[1], b[2], b[3]}; }
DI float swap32_add(float v) { auto r = __builtin_amdgcn_permlane32_swap(__float_as_uint(v), __float_as_uint(v), false, false); return __uint_as_float(r[0]) + __uint_as_float(r[1]); }
DI float swap32_max(float v) { auto r = __builtin_amdgcn_permlane32_swap(__float_as_uint(v), __float_as_uint(v), false, false); return fmaxf(__uint_as_float(r[0]), __uint_as_float(r[1])); }
DI float swap16_add(float v) { auto r = __builtin_amdgcn_permlane16_swap(__float_as_uint(v), __float_as_uint(v), false, false); return __uint_as_float(r[0]) + __uint_as_float(r[1]); }

template <int I, int N, class Fn> DI void static_for(Fn&& f) { if constexpr (I < N) { f(std::integral_constant<int, I>{}); static_for<I + 1, N>(f); } }

template <int P, int NS, int NK, int STAGE>
DI void loader_loop(const unsigned (&voff)[P], const char* gbase, unsigned gstep, unsigned ldo) {
  static_for<0, NS - 1>([&](auto s) {
    if constexpr (s < NK) {
      const char* g = gbase + (size_t)s * gstep;
#pragma unroll
      for (int j = 0; j < P; j++) dma16(voff[j], g, ldo + s * STAGE + j * 1024);
    }
  });
  static_for<0, NK>([&](auto s) {
    constexpr int after = (NK - 1 - s) < (NS - 2) ? (NK - 1 - s) : (NS - 2);
    wait_vm<after * P>();
    bar();
    if constexpr (s + NS - 1 < NK) {
      const char* g = gbase + (size_t)(s + NS - 1) * gstep;
      constexpr int slot = (s + NS - 1) % NS;
#pragma unroll
      for (int j = 0; j < P; j++) dma16(voff[j], g, ldo + slot * STAGE + j * 1024);
    }
  });
}

DI void ln_stats(const float* __restrict__ st, int token, float& mean, float& rstd) {
  const f32x4* p = (const f32x4*)(st + (size_t)token * 16);
  f32x4 a = p[0], b = p[1], c = p[2], d = p[3];
  float m = (a[0] + a[2] + b[0] + b[2] + c[0] + c[2] + d[0] + d[2]) * 0.125f;
  float m2 = a[1] + a[3] + b[1] + b[3] + c[1] + c[3] + d[1] + d[3];
  float e0 = a[0] - m, e1 = a[2] - m, e2 = b[0] - m, e3 = b[2] - m, e4 = c[0] - m, e5 = c[2] - m, e6 = d[0] - m, e7 = d[2] - m;
  m2 += 80.f * (e0 * e0 + e1 * e1 + e2 * e2 + e3 * e3 + e4 * e4 + e5 * e5 + e6 * e6 + e7 * e7);
  mean = m;
  rstd = 1.0f / sqrtf(m2 * (1.0f / 640.f) + LN_EPS);
}

template <int NFF, int NTF, int WP, int STAGE, int NS, int NK>
DI void consumer32(f32x16 (&acc)[NFF][NTF], unsigned xbase, const unsigned (&wbase)[NFF]) {
  bf16x8 w0[NFF], x0[NTF], w1[NFF], x1[NTF];
  auto ld = [&](bf16x8(&w)[NFF], bf16x8(&x)[NTF], unsigned so, int kk) {
#pragma unroll
    for (int a = 0; a < NFF; a++) {
      unsigned p = wbase[a] + so;
      w[a] = cat(lds_rdtr(p + (16 * kk) * WP), lds_rdtr(p + (16 * kk + 4) * WP));
    }
#pragma unroll
    for (int b = 0; b < NTF; b++) x[b] = lds_rd128(((xbase + so) ^ (kk << 5)) + b * 4096);
  };
  auto mm = [&](bf16x8(&w)[NFF], bf16x8(&x)[NTF]) {
#pragma unroll
    for (int a = 0; a < NFF; a++)
#pragma unroll
      for (int b = 0; b < NTF; b++) acc[a][b] = __builtin_amdgcn_mfma_f32_32x32x16_bf16(w[a], x[b], acc[a][b], 0, 0, 0);
  };
  bar();
  unsigned so = 0;
  ld(w0, x0, so, 0);
  int slot = 0;
#pragma unroll 1
  for (int s = 0; s < NK; s++) {
    ld(w1, x1, so, 1); mm(w0, x0); sbar();
    ld(w0, x0, so, 2); mm(w1, x1); sbar();
    ld(w1, x1, so, 3); mm(w0, x0); sbar();
    wait_lgkm_bar(); sbar();
    slot = (slot + 1 == NS) ? 0 : slot + 1;
    so = slot * STAGE;
    if (s + 1 < NK) ld(w0, x0, so, 0);
    mm(w1, x1); sbar();
  }
}

constexpr int QKV_STAGE = 32768, QKV_NS = 4, QKV_LDS = QKV_STAGE * QKV_NS;
__global__ __launch_bounds__(512, 2) void k_qkv(const u16* __restrict__ X, const u16* __restrict__ W, const float* __restrict__ st,
                                                 const float* __restrict__ cs, u16* __restrict__ QKV) {
  extern __shared__ __attribute__((aligned(16))) char smem[];
  const unsigned lb = (unsigned)(size_t)(LAS char*)smem;
  const int tid = threadIdx.x, l = tid & 63, w = __builtin_amdgcn_readfirstlane(tid >> 6);
  const int bid = blockIdx.x, xcd = bid & 7, j = bid >> 3;
  const int rb = 2 * xcd + j / 15, ct = j % 15;
  const int t0 = rb * 128, f0 = ct * 128;
  f32x16 acc[2][2];
  float mean[2], rstd[2];
  f32x4 csv[2][4];
  const int wf = w & 1, wt = (w >> 1) & 1, h = l >> 5;
  if (w >= 4) {
    const int lw = w - 4;
    unsigned voff[8];
    if (lw < 2) {
#pragma unroll
      for (int jj = 0; jj < 8; jj++) {
        int row = (lw * 8 + jj) * 8 + (l >> 3), c = (l & 7) ^ ((row >> 1) & 7);
        voff[jj] = row * (D_ * 2) + c * 16;
      }
      loader_loop<8, QKV_NS, 10, QKV_STAGE>(voff, (const char*)(X + (size_t)t0 * D_), 128, lb + lw * 8192);
    } else {
#pragma unroll
      for (int jj = 0; jj < 8; jj++) {
        int row = ((lw - 2) * 8 + jj) * 4 + (l >> 4), cp = l & 15;
        int c = (((cp >> 2) ^ (row & 3)) << 2) | (cp & 3);
        voff[jj] = row * (NQKV * 2) + c * 16;
      }
      loader_loop<8, QKV_NS, 10, QKV_STAGE>(voff, (const char*)(W + f0), 64 * NQKV * 2, lb + 16384 + (lw - 2) * 8192);
    }
    bar();
  } else {
#pragma unroll
    for (int b = 0; b < 2; b++) ln_stats(st, t0 + wt * 64 + b * 32 + (l & 31), mean[b], rstd[b]);
#pragma unroll
    for (int a = 0; a < 2; a++)
#pragma unroll
      for (int q = 0; q < 4; q++) csv[a][q] = *(const f32x4*)(cs + f0 + wf * 64 + a * 32 + q * 8 + h * 4);
#pragma unroll
    for (int a = 0; a < 2; a++)
#pragma unroll
      for (int b = 0; b < 2; b++)
#pragma unroll
        for (int r = 0; r < 16; r++) acc[a][b][r] = 0.f;
    const int trow = wt * 64 + (l & 31);
    const unsigned xbase = lb + trow * 128 + ((h ^ ((trow >> 1) & 7)) << 4);
    const int g = l >> 4, i = l & 15;
    unsigned wbase[2];
#pragma unroll
    for (int a = 0; a < 2; a++)
      wbase[a] = lb + 16384 + (8 * (g >> 1) + (i >> 2)) * 256 + (((wf * 2 + a) ^ ((i >> 2) & 3)) << 6) + (16 * (g & 1) + 4 * (i & 3)) * 2;
    consumer32<2, 2, 256, QKV_STAGE, QKV_NS, 10>(acc, xbase, wbase);
#pragma unroll
    for (int a = 0; a < 2; a++) {
      const float sc = (f0 + wf * 64 + a * 32 < 640) ? QSCALE : 1.0f;
#pragma unroll
      for (int b = 0; b < 2; b++) {
        const float rs = rstd[b] * sc, mr = mean[b];
#pragma unroll
        for (int q = 0; q < 4; q++) {
          float v0 = (acc[a][b][4 * q + 0] - mr * csv[a][q][0]) * rs, v1 = (acc[a][b][4 * q + 1] - mr * csv[a][q][1]) * rs;
          float v2 = (acc[a][b][4 * q + 2] - mr * csv[a][q][2]) * rs, v3 = (acc[a][b][4 * q + 3] - mr * csv[a][q][3]) * rs;
          u32x2 o = {cvtpk(v0, v1), cvtpk(v2, v3)};
          *(LAS u32x2*)(lb + (wt * 64 + b * 32 + (l & 31)) * 272 + (wf * 64 + a * 32 + q * 8 + h * 4) * 2) = o;
        }
      }
    }
  }
  wait_lgkm_bar();
#pragma unroll
  for (int it = 0; it < 4; it++) {
    int idx = it * 512 + tid, row = idx >> 4, c = idx & 15;
    u32x4 v = *(const LAS u32x4*)(lb + row * 272 + c * 16);
    int col = f0 + c * 8, which = col / 640, cc = col - which * 640, hp = cc / 40, wi = cc - hp * 40;
    *(u32x4*)(QKV + ((size_t)(which * 16 + hp) * S_ + t0 + row) * 40 + wi) = v;
  }
}

constexpr int W1_STAGE = 16384 + 12288, W1_NS = 5, W1_LDS = W1_STAGE * W1_NS;
__global__ __launch_bounds__(512, 2) void k_w1(const u16* __restrict__ X, const u16* __restrict__ W, const float* __restrict__ st,
                                                const float* __restrict__ cs, const u16* __restrict__ b1, u16* __restrict__ H) {
  extern __shared__ __attribute__((aligned(16))) char smem[];
  const unsigned lb = (unsigned)(size_t)(LAS char*)smem;
  const int tid = threadIdx.x, l = tid & 63, w = __builtin_amdgcn_readfirstlane(tid >> 6);
  const int bid = blockIdx.x, xcd = bid & 7, j = bid >> 3;
  const int rb = 2 * xcd + (j >> 4), ct = j & 15;
  const int t0 = rb * 128, f0 = ct * 96;
  const int h = l >> 5;
  if (w >= 4) {
    const int lw = w - 4;
    if (lw < 2) {
      unsigned voff[8];
#pragma unroll
      for (int jj = 0; jj < 8; jj++) {
        int row = (lw * 8 + jj) * 8 + (l >> 3), c = (l & 7) ^ ((row >> 1) & 7);
        voff[jj] = row * (D_ * 2) + c * 16;
      }
      loader_loop<8, W1_NS, 10, W1_STAGE>(voff, (const char*)(X + (size_t)t0 * D_), 128, lb + lw * 8192);
    } else {
      unsigned voff[6];
#pragma unroll
      for (int jj = 0; jj < 6; jj++) {
        int off = ((lw - 2) * 6 + jj) * 1024 + l * 16, row = off / 192, c = (off - row * 192) >> 4;
        voff[jj] = row * (F_ * 2) + c * 16;
      }
      loader_loop<6, W1_NS, 10, W1_STAGE>(voff, (const char*)(W + f0), 64 * F_ * 2, lb + 16384 + (lw - 2) * 6144);
    }
    bar();
  } else {
    f32x16 acc[3][1];
    float mean, rstd;
    f32x4 csv[3][4];
    u32x2 bv[3][4];
    ln_stats(st, t0 + w * 32 + (l & 31), mean, rstd);
#pragma unroll
    for (int a = 0; a < 3; a++)
#pragma unroll
      for (int q = 0; q < 4; q++) {
        csv[a][q] = *(const f32x4*)(cs + f0 + a * 32 + q * 8 + h * 4);
        bv[a][q] = *(const u32x2*)(b1 + f0 + a * 32 + q * 8 + h * 4);
      }
#pragma unroll
    for (int a = 0; a < 3; a++)
#pragma unroll
      for (int r = 0; r < 16; r++) acc[a][0][r] = 0.f;
    const int trow = w * 32 + (l & 31);
    const unsigned xbase = lb + trow * 128 + ((h ^ ((trow >> 1) & 7)) << 4);
    const int g = l >> 4, i = l & 15;
    unsigned wbase[3];
#pragma unroll
    for (int a = 0; a < 3; a++) wbase[a] = lb + 16384 + (8 * (g >> 1) + (i >> 2)) * 192 + a * 64 + (16 * (g & 1) + 4 * (i & 3)) * 2;
    consumer32<3, 1, 192, W1_STAGE, W1_NS, 10>(acc, xbase, wbase);
#pragma unroll
    for (int a = 0; a < 3; a++)
#pragma unroll
      for (int q = 0; q < 4; q++) {
        float v0 = (acc[a][0][4 * q + 0] - mean * csv[a][q][0]) * rstd + bflo(bv[a][q][0]);
        float v1 = (acc[a][0][4 * q + 1] - mean * csv[a][q][1]) * rstd + bfhi(bv[a][q][0]);
        float v2 = (acc[a][0][4 * q + 2] - mean * csv[a][q][2]) * rstd + bflo(bv[a][q][1]);
        float v3 = (acc[a][0][4 * q + 3] - mean * csv[a][q][3]) * rstd + bfhi(bv[a][q][1]);
        v0 = fmaxf(v0, 0.f); v1 = fmaxf(v1, 0.f); v2 = fmaxf(v2, 0.f); v3 = fmaxf(v3, 0.f);
        u32x2 o = {cvtpk(v0 * v0, v1 * v1), cvtpk(v2 * v2, v3 * v3)};
        *(LAS u32x2*)(lb + trow * 208 + (a * 32 + q * 8 + h * 4) * 2) = o;
      }
  }
  wait_lgkm_bar();
#pragma unroll
  for (int it = 0; it < 3; it++) {
    int idx = it * 512 + tid, row = idx / 12, c = idx - row * 12;
    u32x4 v = *(const LAS u32x4*)(lb + row * 208 + c * 16);
    *(u32x4*)(H + (size_t)(t0 + row) * F_ + f0 + c * 8) = v;
  }
}

constexpr int R_STAGE = 8192 + 10240, R_NS = 7, R_RES = R_STAGE * R_NS, R_LDS = R_RES + 10240;
template <int K, bool BIAS>
__global__ __launch_bounds__(512, 2) void k_res(const u16* __restrict__ A, const u16* __restrict__ W, const u16* __restrict__ bias,
                                                 const u16* res, u16* out, float* __restrict__ st) {
  extern __shared__ __attribute__((aligned(16))) char smem[];
  constexpr int NK = K / 64;
  const unsigned lb = (unsigned)(size_t)(LAS char*)smem;
  const int tid = threadIdx.x, l = tid & 63, w = __builtin_amdgcn_readfirstlane(tid >> 6);
  const int bid = blockIdx.x, xcd = bid & 7, j = bid >> 3;
  const int rb = 4 * xcd + (j >> 3), ct = j & 7;
  const int t0 = rb * 64, f0 = ct * 80;
  if (w >= 4) {
    const int lw = w - 4;
    if (lw < 2) {
      unsigned voff[4];
#pragma unroll
      for (int jj = 0; jj < 4; jj++) {
        int row = (lw * 4 + jj) * 8 + (l >> 3), c = (l & 7) ^ ((row >> 1) & 7);
        voff[jj] = row * (K * 2) + c * 16;
      }
      loader_loop<4, R_NS, NK, R_STAGE>(voff, (const char*)(A + (size_t)t0 * K), 128, lb + lw * 4096);
    } else {
      unsigned voff[5];
#pragma unroll
      for (int jj = 0; jj < 5; jj++) {
        int off = ((lw - 2) * 5 + jj) * 1024 + l * 16, row = off / 160, c = (off - row * 160) >> 4;
        dma16(row * (D_ * 2) + c * 16, (const char*)(res + (size_t)t0 * D_ + f0), lb + R_RES + ((lw - 2) * 5 + jj) * 1024);
        int k = 32 * (row >> 5) + 8 * ((row >> 2) & 3) + 4 * ((row >> 4) & 1) + (row & 3);
        voff[jj] = k * (D_ * 2) + c * 16;
      }
      loader_loop<5, R_NS, NK, R_STAGE>(voff, (const char*)(W + f0), 64 * D_ * 2, lb + 8192 + (lw - 2) * 5120);
    }
    bar();
    bar();
  } else {
    const int wt = w & 1, wk = w >> 1, g = l >> 4, i = l & 15;
    f32x4 acc0[5], acc1[5];
    u32x2 bv[5];
    if (BIAS) {
#pragma unroll
      for (int m = 0; m < 5; m++) bv[m] = *(const u32x2*)(bias + f0 + m * 16 + g * 4);
    }
#pragma unroll
    for (int m = 0; m < 5; m++) { acc0[m] = (f32x4){0, 0, 0, 0}; acc1[m] = (f32x4){0, 0, 0, 0}; }
    const int trow = wt * 32 + i;
    const unsigned xbase = (lb + trow * 128 + ((g ^ ((trow >> 1) & 7)) << 4)) ^ (wk << 6);
    const unsigned wbase = lb + 8192 + (32 * wk + 4 * g + (i >> 2)) * 160 + (4 * (i & 3)) * 2;
    bf16x8 wa[5], xa[2], wb[5], xb[2];
    auto ld = [&](bf16x8(&wr)[5], bf16x8(&xr)[2], unsigned so) {
#pragma unroll
      for (int m = 0; m < 5; m++) wr[m] = cat(lds_rdtr(wbase + so + m * 32), lds_rdtr(wbase + so + m * 32 + 16 * 160));
      xr[0] = lds_rd128(xbase + so);
      xr[1] = lds_rd128(xbase + so + 2048);
    };
    auto mm = [&](bf16x8(&wr)[5], bf16x8(&xr)[2]) {
#pragma unroll
      for (int m = 0; m < 5; m++) {
        acc0[m] = __builtin_amdgcn_mfma_f32_16x16x32_bf16(wr[m], xr[0], acc0[m], 0, 0, 0);
        acc1[m] = __builtin_amdgcn_mfma_f32_16x16x32_bf16(wr[m], xr[1], acc1[m], 0, 0, 0);
      }
    };
    bar();
    ld(wa, xa, 0);
    int slot = 0;
#pragma unroll 1
    for (int s = 0; s < NK; s += 2) {
      wait_lgkm_bar(); sbar();
      slot = (slot + 1 == R_NS) ? 0 : slot + 1;
      ld(wb, xb, slot * R_STAGE);
      mm(wa, xa); sbar();
      wait_lgkm_bar(); sbar();
      slot = (slot + 1 == R_NS) ? 0 : slot + 1;
      if (s + 2 < NK) ld(wa, xa, slot * R_STAGE);
      mm(wb, xb); sbar();
    }
#pragma unroll
    for (int m = 0; m < 5; m++) {
      f32x4 snd, kp;
#pragma unroll
      for (int e = 0; e < 4; e++) { snd[e] = wk ? acc0[m][e] : acc1[m][e]; kp[e] = wk ? acc1[m][e] : acc0[m][e]; }
      *(LAS f32x4*)(lb + ((wt * 2 + wk) * 5 + m) * 1024 + l * 16) = snd;
      acc0[m] = kp;
    }
    wait_lgkm_bar();
    const int tok = wt * 32 + wk * 16 + i;
    float v[5][4];
    float sum = 0.f;
#pragma unroll
    for (int m = 0; m < 5; m++) {
      f32x4 o = *(const LAS f32x4*)(lb + ((wt * 2 + (1 - wk)) * 5 + m) * 1024 + l * 16);
      f32x4 a = acc0[m];
      const unsigned ra = lb + R_RES + tok * 160 + (m * 16 + g * 4) * 2;
      u32x2 r = *(const LAS u32x2*)ra;
      float y0 = a[0] + o[0] + bflo(r[0]), y1 = a[1] + o[1] + bfhi(r[0]), y2 = a[2] + o[2] + bflo(r[1]), y3 = a[3] + o[3] + bfhi(r[1]);
      if (BIAS) { y0 += bflo(bv[m][0]); y1 += bfhi(bv[m][0]); y2 += bflo(bv[m][1]); y3 += bfhi(bv[m][1]); }
      u32x2 pk = {cvtpk(y0, y1), cvtpk(y2, y3)};
      *(LAS u32x2*)ra = pk;
      v[m][0] = bflo(pk[0]); v[m][1] = bfhi(pk[0]); v[m][2] = bflo(pk[1]); v[m][3] = bfhi(pk[1]);
      sum += (v[m][0] + v[m][1]) + (v[m][2] + v[m][3]);
    }
    sum = swap32_add(swap16_add(sum));
    const float mb = sum * (1.0f / 80.f);
    float m2 = 0.f;
#pragma unroll
    for (int m = 0; m < 5; m++)
#pragma unroll
      for (int r = 0; r < 4; r++) { float d = v[m][r] - mb; m2 += d * d; }
    m2 = swap32_add(swap16_add(m2));
    if (g == 0) *(f32x2*)(st + (size_t)(t0 + tok) * 16 + ct * 2) = (f32x2){mb, m2};
  }
  wait_lgkm_bar();
  for (int idx = tid; idx < 640; idx += 512) {
    int row = idx / 10, c = idx - row * 10;
    u32x4 v = *(const LAS u32x4*)(lb + R_RES + row * 160 + c * 16);
    *(u32x4*)(out + (size_t)(t0 + row) * D_ + f0 + c * 8) = v;
  }
}

constexpr int AT_K = 0, AT_V = 30720, AT_Q = 61440, AT_C = 71680, AT_O = AT_C + 2048, AT_LDS = AT_O + 10240;
__global__ __launch_bounds__(512, 2) void k_attn(const u16* __restrict__ QKV, u16* __restrict__ O) {
  extern __shared__ __attribute__((aligned(16))) char smem[];
  const unsigned lb = (unsigned)(size_t)(LAS char*)smem;
  const int tid = threadIdx.x, l = tid & 63, w = __builtin_amdgcn_readfirstlane(tid >> 6);
  const int bid = blockIdx.x, xcd = bid & 7, j = bid >> 3;
  const int rb = 2 * xcd + (j >> 4), hp = j & 15;
  const int q0 = rb * 128;
  const int skip = (q0 >= 256) ? 0 : (256 - q0) * 80 / 1024;
  const char* Qg = (const char*)(QKV + ((size_t)(0 * 16 + hp) * S_ + q0) * 40);
  const char* Kg = (const char*)(QKV + ((size_t)(1 * 16 + hp) * S_) * 40) + (ptrdiff_t)(q0 - 256) * 80;
  const char* Vg = (const char*)(QKV + ((size_t)(2 * 16 + hp) * S_) * 40) + (ptrdiff_t)(q0 - 256) * 80;
  const unsigned voff = l * 16;
  for (int p = w; p < 10; p += 8) dma16(voff, Qg + p * 1024, lb + AT_Q + p * 1024);
  for (int p = w; p < 30; p += 8) if (p >= skip) dma16(voff, Kg + p * 1024, lb + AT_K + p * 1024);
  for (int p = w; p < 30; p += 8) if (p >= skip) dma16(voff, Vg + p * 1024, lb + AT_V + p * 1024);
  if (tid < 4) *(LAS u32x2*)(lb + AT_C + tid * 640) = (u32x2){0x00003f80u, 0u};
  wait_vm<0>();
  wait_lgkm_bar();
  const int head = w & 1, qt = w >> 1, h = l >> 5, a = l & 31;
  bf16x8 q0f, q1f;
  {
    const unsigned qa = lb + AT_Q + (qt * 32 + a) * 80 + head * 40;
    q0f = cat(lds_rd64(qa + h * 16), lds_rd64(qa + h * 16 + 8));
    s16x4 t = lds_rd64(qa + 32);
    s16x4 z = {0, 0, 0, 0};
    q1f = cat(h ? z : t, z);
  }
  const int tfirst = (q0 + qt * 32 >= 256) ? 0 : (256 - (q0 + qt * 32)) / 32;
  f32x16 S[9];
  const unsigned ka = lb + AT_K + (qt * 32 + a) * 80 + head * 40;
  const float NEG = -__builtin_inff();
  static_for<0, 9>([&](auto t) {
    if (t >= tfirst) {
      const unsigned p = ka + t * 2560;
      bf16x8 k0 = cat(lds_rd64(p + h * 16), lds_rd64(p + h * 16 + 8));
      s16x4 z = {0, 0, 0, 0};
      bf16x8 k1 = cat(lds_rd64(p + 32), z);
      f32x16 s;
#pragma unroll
      for (int r = 0; r < 16; r++) s[r] = 0.f;
      s = __builtin_amdgcn_mfma_f32_32x32x16_bf16(k0, q0f, s, 0, 0, 0);
      s = __builtin_amdgcn_mfma_f32_32x32x16_bf16(k1, q1f, s, 0, 0, 0);
      if constexpr (t == 0) {
#pragma unroll
        for (int r = 0; r < 16; r++) { int b = (r & 3) + 8 * (r >> 2) + 4 * h; s[r] = (b > a) ? s[r] : NEG; }
      }
      if constexpr (t == 8) {
#pragma unroll
        for (int r = 0; r < 16; r++) { int b = (r & 3) + 8 * (r >> 2) + 4 * h; s[r] = (b <= a) ? s[r] : NEG; }
      }
      S[t] = s;
    } else {
#pragma unroll
      for (int r = 0; r < 16; r++) S[t][r] = NEG;
    }
  });
  float m = NEG;
#pragma unroll
  for (int t = 0; t < 9; t++)
#pragma unroll
    for (int r = 0; r < 16; r++) m = fmaxf(m, S[t][r]);
  m = swap32_max(m);
  f32x16 o;
#pragma unroll
  for (int r = 0; r < 16; r++) o[r] = 0.f;
  const int g = l >> 4, i = l & 15;
  const bool cst = (g & 1) && (i & 3) >= 1;
  const unsigned vstride = cst ? 0u : 2560u;
  unsigned va = cst ? (lb + AT_C) : (lb + AT_V + (qt * 32 + 4 * (g >> 1) + (i >> 2)) * 80 + head * 40 + (16 * (g & 1) + 4 * (i & 3)) * 2);
  static_for<0, 9>([&](auto t) {
    if (t >= tfirst) {
      unsigned pk[8];
#pragma unroll
      for (int r = 0; r < 8; r++)
        pk[r] = cvtpk(__builtin_amdgcn_exp2f(S[t][2 * r] - m), __builtin_amdgcn_exp2f(S[t][2 * r + 1] - m));
      const unsigned p = va + t * vstride;
      bf16x8 v0 = cat(lds_rdtr(p), lds_rdtr(p + 8 * 80));
      bf16x8 v1 = cat(lds_rdtr(p + 16 * 80), lds_rdtr(p + 24 * 80));
      u32x4 p0 = {pk[0], pk[1], pk[2], pk[3]}, p1 = {pk[4], pk[5], pk[6], pk[7]};
      o = __builtin_amdgcn_mfma_f32_32x32x16_bf16(v0, __builtin_bit_cast(bf16x8, p0), o, 0, 0, 0);
      o = __builtin_amdgcn_mfma_f32_32x32x16_bf16(v1, __builtin_bit_cast(bf16x8, p1), o, 0, 0, 0);
    }
  });
  float rsum;
  {
    auto r = __builtin_amdgcn_permlane32_swap(__float_as_uint(o[8]), __float_as_uint(o[8]), false, false);
    rsum = __uint_as_float(r[1]);
  }
  const float inv = 1.0f / rsum;
  const unsigned oa = lb + AT_O + (qt * 32 + a) * 80 + head * 40 + h * 8;
  *(LAS u32x2*)(oa) = (u32x2){cvtpk(o[0] * inv, o[1] * inv), cvtpk(o[2] * inv, o[3] * inv)};
  *(LAS u32x2*)(oa + 16) = (u32x2){cvtpk(o[4] * inv, o[5] * inv), cvtpk(o[6] * inv, o[7] * inv)};
  if (h == 0) *(LAS u32x2*)(oa + 32) = (u32x2){cvtpk(o[8] * inv, o[9] * inv), cvtpk(o[10] * inv, o[11] * inv)};
  wait_lgkm_bar();
  for (int idx = tid; idx < 640; idx += 512) {
    int row = idx / 5, c = idx - row * 5;
    u32x4 v = *(const LAS u32x4*)(lb + AT_O + row * 80 + c * 16);
    *(u32x4*)(O + (size_t)(q0 + row) * D_ + hp * 40 + c * 8) = v;
  }
}

__global__ __launch_bounds__(256) void k_prep(const u16* __restrict__ X, const u16* __restrict__ Wqkv, const u16* __restrict__ W1,
                                              float* __restrict__ st, float* __restrict__ csq, float* __restrict__ cs1) {
  __shared__ float red[32][65];
  const int bid = blockIdx.x, tid = threadIdx.x;
  if (bid < 54) {
    const bool isq = bid < 30;
    const u16* W = isq ? Wqkv : W1;
    const int ld = isq ? NQKV : F_;
    const int c0 = (isq ? bid : bid - 30) * 64;
    const int c8 = tid & 7, rg = tid >> 3;
    float s[8] = {0, 0, 0, 0, 0, 0, 0, 0};
#pragma unroll 4
    for (int r = rg; r < 640; r += 32) {
      u32x4 v = *(const u32x4*)(W + (size_t)r * ld + c0 + c8 * 8);
#pragma unroll
      for (int e = 0; e < 4; e++) { s[2 * e] += bflo(v[e]); s[2 * e + 1] += bfhi(v[e]); }
    }
#pragma unroll
    for (int e = 0; e < 8; e++) red[rg][c8 * 8 + e] = s[e];
    __syncthreads();
    if (tid < 64) {
      float t = 0.f;
      for (int r = 0; r < 32; r++) t += red[r][tid];
      (isq ? csq : cs1)[c0 + tid] = t;
    }
  } else {
    const int idx = (bid - 54) * 256 + tid, tok = idx >> 3, b = idx & 7;
    const u32x4* p = (const u32x4*)(X + (size_t)tok * D_ + b * 80);
    float v[80];
    float sum = 0.f;
#pragma unroll
    for (int c = 0; c < 10; c++) {
      u32x4 u = p[c];
#pragma unroll
      for (int e = 0; e < 4; e++) { v[c * 8 + 2 * e] = bflo(u[e]); v[c * 8 + 2 * e + 1] = bfhi(u[e]); }
    }
#pragma unroll
    for (int e = 0; e < 80; e++) sum += v[e];
    const float mb = sum * (1.0f / 80.f);
    float m2 = 0.f;
#pragma unroll
    for (int e = 0; e < 80; e++) { float d = v[e] - mb; m2 += d * d; }
    *(f32x2*)(st + (size_t)tok * 16 + b * 2) = (f32x2){mb, m2};
  }
}

extern "C" void kernel_launch(void* const* d_in, const int* in_sizes, int n_in, void* d_out, int out_size, void* d_ws, size_t ws_size,
                              hipStream_t stream) {
  const u16* x = (const u16*)d_in[0];
  const u16* wqkv = (const u16*)d_in[1];
  const u16* wo = (const u16*)d_in[2];
  const u16* w1 = (const u16*)d_in[4];
  const u16* b1 = (const u16*)d_in[5];
  const u16* w2 = (const u16*)d_in[6];
  const u16* b2 = (const u16*)d_in[7];
  char* ws = (char*)d_ws;
  float* st = (float*)(ws);
  float* csq = (float*)(ws + 131072);
  float* cs1 = (float*)(ws + 131072 + 8192);
  u16* qkv = (u16*)(ws + 0x40000);
  u16* o = (u16*)(ws + 0x40000 + 0x800000);
  u16* xs = (u16*)(ws + 0x40000 + 0x800000 + 0x300000);
  u16* hb = (u16*)(ws + 0x40000 + 0x800000 + 0x600000);
  u16* out = (u16*)d_out;
  hipFuncSetAttribute((const void*)k_qkv, hipFuncAttributeMaxDynamicSharedMemorySize, QKV_LDS);
  hipFuncSetAttribute((const void*)k_w1, hipFuncAttributeMaxDynamicSharedMemorySize, W1_LDS);
  hipFuncSetAttribute((const void*)k_res<640, false>, hipFuncAttributeMaxDynamicSharedMemorySize, R_LDS);
  hipFuncSetAttribute((const void*)k_res<1536, true>, hipFuncAttributeMaxDynamicSharedMemorySize, R_LDS);
  hipFuncSetAttribute((const void*)k_attn, hipFuncAttributeMaxDynamicSharedMemorySize, AT_LDS);
  k_prep<<<54 + 64, 256, 0, stream>>>(x, wqkv, w1, st, csq, cs1);
  for (int layer = 0; layer < 2; layer++) {
    const u16* xin = layer == 0 ? x : xs;
    k_qkv<<<240, 512, QKV_LDS, stream>>>(xin, wqkv, st, csq, qkv);
    k_attn<<<256, 512, AT_LDS, stream>>>(qkv, o);
    k_res<640, false><<<256, 512, R_LDS, stream>>>(o, wo, nullptr, xin, xs, st);
    k_w1<<<256, 512, W1_LDS, stream>>>(xs, w1, st, cs1, b1, hb);
    k_res<1536, true><<<256, 512, R_LDS, stream>>>(hb, w2, b2, xs, layer == 0 ? xs : out, st);
  }
}
```

```cpp
#include <hip/hip_runtime.h>
#include <stdint.h>
#include <type_traits>

typedef short bf16x8 __attribute__((ext_vector_type(8)));
typedef short s16x4 __attribute__((ext_vector_type(4)));
typedef float f32x2 __attribute__((ext_vector_type(2)));
typedef float f32x4 __attribute__((ext_vector_type(4)));
typedef float f32x16 __attribute__((ext_vector_type(16)));
typedef unsigned u32x2 __attribute__((ext_vector_type(2)));
typedef unsigned u32x4 __attribute__((ext_vector_type(4)));
typedef __bf16 bf16x2_t __attribute__((ext_vector_type(2)));
typedef unsigned short u16;
typedef unsigned long long u64;
#define LAS __attribute__((address_space(3)))
#define GAS __attribute__((address_space(1)))
#define DI __device__ __forceinline__

constexpr float LN_EPS = 1e-5f;
constexpr float QSCALE = 0.22360679774997896f * 1.4426950408889634f;

constexpr unsigned WS_FLAG = 0x1000;
constexpr unsigned WS_ST = 0x60000;
constexpr unsigned WS_CSQ = 0xE0000;
constexpr unsigned WS_CS1 = 0xF4000;
constexpr unsigned WS_WQ = 0x110000;
constexpr unsigned WS_W1 = 0x368000;
constexpr unsigned WS_WO = 0x548000;
constexpr unsigned WS_W2 = 0x610000;
constexpr unsigned WS_KV = 0x800000;
constexpr unsigned WS_O = 0x1200000;
constexpr unsigned WS_X1 = 0x1700000;
constexpr unsigned WS_X2 = 0x1C00000;
constexpr unsigned WS_H = 0x1F00000;
constexpr unsigned WS_DBG = 0x3000000;
constexpr unsigned L_K = 0, L_V = 30720, L_Q = 61440;
constexpr unsigned L_CSQ = 157696, L_CS1 = 158208, L_B1 = 158720, L_B2 = 159232, L_MS = 159744, L_CNT = 160768, L_ONE = 161024;
constexpr unsigned LDS_BYTES = 163840;
#define SPINMAX 400000
constexpr int SPIN_MAX = SPINMAX;

DI unsigned cvtpk(float lo, float hi) { f32x2 v = {lo, hi}; bf16x2_t b = __builtin_convertvector(v, bf16x2_t); return __builtin_bit_cast(unsigned, b); }
DI float bflo(unsigned u) { return __uint_as_float(u << 16); }
DI float bfhi(unsigned u) { return __uint_as_float(u & 0xffff0000u); }
#define XSC " sc1"
#define WSC " sc1"
DI void dma16(unsigned voff, const void* sbase, unsigned lds) {
  asm volatile("s_mov_b32 m0, %2\n\ts_nop 0\n\tglobal_load_lds_dwordx4 %0, %1 sc1" ::"v"(voff), "s"(sbase), "s"(lds) : "memory");
}
template <int IMM> DI void dma4(unsigned v0, unsigned v1, unsigned v2, unsigned v3, const void* sbase, unsigned ldsb) {
  asm volatile("s_add_u32 m0, %5, %6\n\ts_nop 0\n\tglobal_load_lds_dwordx4 %0, %4 " XSC "\n\tglobal_load_lds_dwordx4 %1, %4 offset:1024 " XSC "\n\t"
               "global_load_lds_dwordx4 %2, %4 offset:2048 " XSC "\n\tglobal_load_lds_dwordx4 %3, %4 offset:3072 " XSC
               ::"v"(v0), "v"(v1), "v"(v2), "v"(v3), "s"(sbase), "s"(ldsb), "n"(IMM) : "memory", "scc");
}
template <int IMM> DI void dma2(unsigned v0, unsigned v1, const void* sbase, unsigned ldsb) {
  asm volatile("s_add_u32 m0, %3, %4\n\ts_nop 0\n\tglobal_load_lds_dwordx4 %0, %2 " XSC "\n\tglobal_load_lds_dwordx4 %1, %2 offset:1024 " XSC
               ::"v"(v0), "v"(v1), "s"(sbase), "s"(ldsb), "n"(IMM) : "memory", "scc");
}
template <int IMM> DI void dma3(unsigned v0, const void* sbase, unsigned ldsb) {
  asm volatile("s_add_u32 m0, %2, %3\n\ts_nop 0\n\tglobal_load_lds_dwordx4 %0, %1 " XSC "\n\tglobal_load_lds_dwordx4 %0, %1 offset:1024 " XSC "\n\t"
               "global_load_lds_dwordx4 %0, %1 offset:2048 " XSC
               ::"v"(v0), "s"(sbase), "s"(ldsb), "n"(IMM) : "memory", "scc");
}
template <int IMM, int OFF> DI void dma1(unsigned v0, const void* sbase, unsigned ldsb) {
  asm volatile("s_add_u32 m0, %2, %3\n\ts_nop 0\n\tglobal_load_lds_dwordx4 %0, %1 offset:%4 " XSC
               ::"v"(v0), "s"(sbase), "s"(ldsb), "n"(IMM), "n"(OFF) : "memory", "scc");
}
template <int IMM> DI void wdma4(unsigned v0, unsigned v1, unsigned v2, unsigned v3, const void* sbase, unsigned ldsb) {
  asm volatile("s_add_u32 m0, %5, %6\n\ts_nop 0\n\tglobal_load_lds_dwordx4 %0, %4 " WSC "\n\tglobal_load_lds_dwordx4 %1, %4 offset:1024 " WSC "\n\t"
               "global_load_lds_dwordx4 %2, %4 offset:2048 " WSC "\n\tglobal_load_lds_dwordx4 %3, %4 offset:3072 " WSC
               ::"v"(v0), "v"(v1), "v"(v2), "v"(v3), "s"(sbase), "s"(ldsb), "n"(IMM) : "memory", "scc");
}
template <int IMM> DI void wdma2(unsigned v0, unsigned v1, const void* sbase, unsigned ldsb) {
  asm volatile("s_add_u32 m0, %3, %4\n\ts_nop 0\n\tglobal_load_lds_dwordx4 %0, %2 " WSC "\n\tglobal_load_lds_dwordx4 %1, %2 offset:1024 " WSC
               ::"v"(v0), "v"(v1), "s"(sbase), "s"(ldsb), "n"(IMM) : "memory", "scc");
}
template <int IMM> DI void wdma3(unsigned v0, const void* sbase, unsigned ldsb) {
  asm volatile("s_add_u32 m0, %2, %3\n\ts_nop 0\n\tglobal_load_lds_dwordx4 %0, %1 " WSC "\n\tglobal_load_lds_dwordx4 %0, %1 offset:1024 " WSC "\n\t"
               "global_load_lds_dwordx4 %0, %1 offset:2048 " WSC
               ::"v"(v0), "s"(sbase), "s"(ldsb), "n"(IMM) : "memory", "scc");
}
template <int IMM, int OFF> DI void wdma1(unsigned v0, const void* sbase, unsigned ldsb) {
  asm volatile("s_add_u32 m0, %2, %3\n\ts_nop 0\n\tglobal_load_lds_dwordx4 %0, %1 offset:%4 " WSC
               ::"v"(v0), "s"(sbase), "s"(ldsb), "n"(IMM), "n"(OFF) : "memory", "scc");
}
template <class T> DI void opaque_s(T& v) { asm volatile("" : "+s"(v)); }
template <class T> DI void opaque_v(T& v) { asm volatile("" : "+v"(v)); }
template <int N> DI void wait_vm() { asm volatile("s_waitcnt vmcnt(%0)" ::"n"(N) : "memory"); }
DI void bar() { asm volatile("s_barrier" ::: "memory"); }
DI void wait_lgkm_bar() { asm volatile("s_waitcnt lgkmcnt(0)\n\ts_barrier" ::: "memory"); }
DI void sbar() { __builtin_amdgcn_sched_barrier(0); }
DI bf16x8 lds_rd128(unsigned a) { return *(const LAS bf16x8*)a; }
DI f32x4 lds_rdf4(unsigned a) { return *(const LAS f32x4*)a; }
DI u32x4 lds_rdu4(unsigned a) { return *(const LAS u32x4*)a; }
DI s16x4 lds_rd64(unsigned a) { return *(const LAS s16x4*)a; }
DI s16x4 lds_rdtr(unsigned a) { return __builtin_amdgcn_ds_read_tr16_b64_v4i16((LAS s16x4*)a); }
DI bf16x8 cat(s16x4 a, s16x4 b) { return (bf16x8){a[0], a[1], a[2], a[3], b[0], b[1], b[2], b[3]}; }
DI float swap32_add(float v) { auto r = __builtin_amdgcn_permlane32_swap(__float_as_uint(v), __float_as_uint(v), false, false); return __uint_as_float(r[0]) + __uint_as_float(r[1]); }
DI float swap32_max(float v) { auto r = __builtin_amdgcn_permlane32_swap(__float_as_uint(v), __float_as_uint(v), false, false); return fmaxf(__uint_as_float(r[0]), __uint_as_float(r[1])); }
DI float swap16_add(float v) { auto r = __builtin_amdgcn_permlane16_swap(__float_as_uint(v), __float_as_uint(v), false, false); return __uint_as_float(r[0]) + __uint_as_float(r[1]); }
template <int I, int N, class Fn> DI void static_for(Fn&& f) { if constexpr (I < N) { f(std::integral_constant<int, I>{}); static_for<I + 1, N>(f); } }
typedef __amdgpu_buffer_rsrc_t rsrc_t;
DI void st16(rsrc_t r, unsigned voff, unsigned soff, u32x4 v) { __builtin_amdgcn_raw_buffer_store_b128(v, r, voff, soff, 16); }
DI void st8(rsrc_t r, unsigned voff, unsigned soff, u32x2 v) { __builtin_amdgcn_raw_buffer_store_b64(v, r, voff, soff, 16); }
DI void st4(rsrc_t r, unsigned voff, unsigned soff, unsigned v) { __builtin_amdgcn_raw_buffer_store_b32(v, r, voff, soff, 16); }
DI unsigned ld4(rsrc_t r, unsigned voff, unsigned soff) { return __builtin_amdgcn_raw_buffer_load_b32(r, voff, soff, 16); }
DI void sw32(unsigned& a, unsigned& b) { auto r = __builtin_amdgcn_permlane32_swap(a, b, false, false); a = r[0]; b = r[1]; }
DI void sw16(unsigned& a, unsigned& b) { auto r = __builtin_amdgcn_permlane16_swap(a, b, false, false); a = r[0]; b = r[1]; }

#define TS(id)
#define FARG
#define FPASS(ph)
#define TSF(k)
#define TSFC(k)
#define TSC(id) TS(id)
#define TSB(k) TS(tb + (k))
#define FINEL 1

DI void poll(const char* ws, unsigned off, bool active, u64 E) {
  const GAS u64* p = (const GAS u64*)(ws + off);
  for (int it = 0; it < SPIN_MAX; it++) {
    u64 v = E;
    if (active) v = __hip_atomic_load(p, __ATOMIC_RELAXED, __HIP_MEMORY_SCOPE_AGENT);
    if (__builtin_amdgcn_ballot_w64(v != E) == 0) break;
    __builtin_amdgcn_s_sleep(1);
  }
}
template <int NW> DI void publish(char* ws, unsigned flag_off, unsigned cnt_lds, u64 E, int l) {
  wait_vm<0>();
  if (l == 0) {
    unsigned old = __hip_atomic_fetch_add((LAS unsigned*)cnt_lds, 1u, __ATOMIC_RELAXED, __HIP_MEMORY_SCOPE_WORKGROUP);
    if ((old & (NW - 1)) == NW - 1) __hip_atomic_store((GAS u64*)(ws + flag_off), E, __ATOMIC_RELAXED, __HIP_MEMORY_SCOPE_AGENT);
  }
}
DI unsigned flag_off(int edge, int b) { return WS_FLAG + (unsigned)(edge * 256 + b) * 128u; }

template <int NS, int NK, bool STATS>
DI void xloader(unsigned grp_stride, const char* g, unsigned gstep, unsigned ldo, const char* stg, unsigned ms_addr, int l FARG) {
  constexpr int P = 8, ST = 16384;
  unsigned v0 = l * 16; opaque_v(v0);
  unsigned v1 = v0 + grp_stride;
  opaque_s(g); opaque_s(ldo);
  auto stage = [&](auto slot) {
    dma4<slot * ST>(v0, v0, v0, v0, g, ldo);
    dma4<slot * ST + 4096>(v1, v1, v1, v1, g, ldo);
    g += gstep; opaque_s(g);
  };
  TSF(58);
  static_for<0, NS>([&](auto s) { stage(s); });
  TSF(59);
  wait_vm<(NS - 1) * P>();
  TSF(14);
  bar();
  TSF(0);
  u32x2 sv[8];
  if constexpr (STATS) {
    const unsigned so = l * 8;
#pragma unroll
    for (int c = 0; c < 8; c++) asm volatile("global_load_dwordx2 %0, %1, %2 offset:%3 sc1" : "=v"(sv[c]) : "v"(so), "s"(stg + (c >> 2) * 2048), "n"((c & 3) * 512) : "memory");
  }
  static_for<1, NK + 1>([&](auto s) {
    if constexpr (s < NK) {
      constexpr int issued = (NS + s - 1) < NK ? (NS + s - 1) : NK;
      constexpr int allowed = (issued - (s + 1)) * P + ((STATS && s < NS) ? 8 : 0);
      wait_vm<allowed>();
    }
    TSF(14 + s);
    if constexpr (STATS && s == NS + 1) wait_lgkm_bar(); else bar();
    TSF(s);
    if constexpr (s - 1 + NS < NK) stage(std::integral_constant<int, (s - 1 + NS) % NS>{});
    if constexpr (STATS && s == NS) {
      asm volatile("" : "+v"(sv[0]), "+v"(sv[1]), "+v"(sv[2]), "+v"(sv[3]), "+v"(sv[4]), "+v"(sv[5]), "+v"(sv[6]), "+v"(sv[7]));
      float m = 0.f, m2 = 0.f;
#pragma unroll
      for (int c = 0; c < 8; c++) { m += __uint_as_float(sv[c][0]); m2 += __uint_as_float(sv[c][1]); }
      m *= 0.125f;
      float e2 = 0.f;
#pragma unroll
      for (int c = 0; c < 8; c++) { float e = __uint_as_float(sv[c][0]) - m; e2 += e * e; }
      m2 += 80.f * e2;
      const float rstd = 1.0f / sqrtf(m2 * (1.0f / 640.f) + LN_EPS);
      *(LAS f32x2*)ms_addr = (f32x2){m, rstd};
    }
  });
}
template <int WST> struct WLoad {
  unsigned v0, v1, v2, lds;
  const char* g;
  DI WLoad(const char* g_, unsigned lds_, int l) : lds(lds_), g(g_) {
    v0 = l * 16; opaque_v(v0); v1 = v0 + 4096; v2 = v0 + 8192;
    opaque_s(g); opaque_s(lds);
  }
  template <int SLOT> DI void stage(int l) {
    constexpr int HB = WST / 2;
    wdma4<SLOT * WST>(v0, v0, v0, v0, g, lds);
    if constexpr (HB == 6144) wdma2<SLOT * WST + 4096>(v1, v1, g, lds);
    if constexpr (HB == 7680) { wdma3<SLOT * WST + 4096>(v1, g, lds); if (l < 32) wdma1<SLOT * WST + 4096, 3072>(v1, g, lds); }
    if constexpr (HB == 10240) { wdma4<SLOT * WST + 4096>(v1, v1, v1, v1, g, lds); wdma2<SLOT * WST + 8192>(v2, v2, g, lds); }
    g += WST; opaque_s(g);
  }
};
template <int WST, int NS, int NK> DI void wprefill(const char* g, unsigned lds, int l) {
  WLoad<WST> W(g, lds, l);
  static_for<0, (NS < NK ? NS : NK)>([&](auto s) { W.template stage<s>(l); });
}
template <int WST, int NS, int NK> DI void wloop(const char* g, unsigned lds, int l FARG) {
  constexpr int P = (WST / 2 + 1023) / 1024;
  constexpr int pre = NS < NK ? NS : NK;
  WLoad<WST> W(g + (size_t)pre * WST, lds, l);
  TSF(59);
  wait_vm<(pre - 1) * P>();
  TSF(14);
  bar();
  TSF(0);
  static_for<1, NK + 1>([&](auto s) {
    if constexpr (s < NK) {
      constexpr int issued = (NS + s - 1) < NK ? (NS + s - 1) : NK;
      wait_vm<(issued - (s + 1)) * P>();
    }
    TSF(14 + s);
    bar();
    TSF(s);
    if constexpr (s - 1 + NS < NK) W.template stage<(s - 1 + NS) % NS>(l);
  });
}

template <int NFF, int NTF, int XNS, int WST, int WNS, int NK>
DI void gemm32(f32x16 (&acc)[NFF][NTF], unsigned xbase, unsigned wbase FARG) {
  bf16x8 w0[NFF], x0[NTF], w1[NFF], x1[NTF];
  auto ld = [&](bf16x8(&w)[NFF], bf16x8(&x)[NTF], unsigned xo, unsigned wo, int kk) {
    const unsigned wa = (wbase + wo) ^ (kk << 5), xa = xbase + xo + kk * 1024;
    w[0] = lds_rd128(wa);
#pragma unroll
    for (int b = 0; b < NTF; b++) x[b] = lds_rd128(xa + b * 4096);
#pragma unroll
    for (int a = 1; a < NFF; a++) w[a] = lds_rd128(wa + a * 4096);
  };
  auto mm = [&](bf16x8(&w)[NFF], bf16x8(&x)[NTF]) {
#pragma unroll
    for (int a = 0; a < NFF; a++)
#pragma unroll
      for (int b = 0; b < NTF; b++) acc[a][b] = __builtin_amdgcn_mfma_f32_32x32x16_bf16(w[a], x[b], acc[a][b], 0, 0, 0);
  };
  auto sched = [&]() {
    constexpr int NM = NFF * NTF, ND = NFF + NTF;
    __builtin_amdgcn_sched_group_barrier(0x008, 1, 0);
    __builtin_amdgcn_sched_group_barrier(0x100, (ND + 1) / 2, 0);
    __builtin_amdgcn_sched_group_barrier(0x008, 1, 0);
    __builtin_amdgcn_sched_group_barrier(0x100, ND / 2, 0);
    __builtin_amdgcn_sched_group_barrier(0x008, NM - 2, 0);
    sbar();
  };
  TSFC(14);
  wait_lgkm_bar();
  TSFC(0);
  unsigned xo = 0, wo = 0;
  ld(w0, x0, xo, wo, 0);
#pragma unroll 1
  for (int s = 0; s < NK; s++) {
    ld(w1, x1, xo, wo, 1); mm(w0, x0); sched();
    ld(w0, x0, xo, wo, 2); mm(w1, x1); sched();
    ld(w1, x1, xo, wo, 3); mm(w0, x0); sched();
    TSFC(15 + s);
    wait_lgkm_bar(); sbar();
    TSFC(1 + s);
    xo = (xo + 16384 == XNS * 16384) ? 0 : xo + 16384;
    wo = (wo + WST == WNS * WST) ? 0 : wo + WST;
    ld(w0, x0, xo, wo, 0);
    mm(w1, x1); sched();
  }
}
template <int XNS, int WNS, int NK>
DI void gemm16(f32x4 (&acc0)[5], f32x4 (&acc1)[5], unsigned xb0, unsigned xb1, unsigned wbase FARG) {
  bf16x8 wa[5], xa[2], wb[5], xb[2];
  auto ld = [&](bf16x8(&w)[5], bf16x8(&x)[2], unsigned xo, unsigned wo, int ks) {
    const unsigned wp = (wbase + wo) ^ (ks << 6);
    w[0] = lds_rd128(wp);
    x[0] = lds_rd128(xb0 + xo + ks * 2048);
    x[1] = lds_rd128(xb1 + xo + ks * 2048);
#pragma unroll
    for (int m = 1; m < 5; m++) w[m] = lds_rd128(wp + m * 2048);
  };
  auto mm = [&](bf16x8(&w)[5], bf16x8(&x)[2]) {
#pragma unroll
    for (int m = 0; m < 5; m++) {
      acc0[m] = __builtin_amdgcn_mfma_f32_16x16x32_bf16(w[m], x[0], acc0[m], 0, 0, 0);
      acc1[m] = __builtin_amdgcn_mfma_f32_16x16x32_bf16(w[m], x[1], acc1[m], 0, 0, 0);
    }
  };
  auto sched = [&]() {
    __builtin_amdgcn_sched_group_barrier(0x008, 1, 0);
    __builtin_amdgcn_sched_group_barrier(0x100, 2, 0);
    __builtin_amdgcn_sched_group_barrier(0x008, 2, 0);
    __builtin_amdgcn_sched_group_barrier(0x100, 2, 0);
    __builtin_amdgcn_sched_group_barrier(0x008, 2, 0);
    __builtin_amdgcn_sched_group_barrier(0x100, 3, 0);
    __builtin_amdgcn_sched_group_barrier(0x008, 5, 0);
    sbar();
  };
  TSFC(14);
  bar();
  TSFC(0);
  unsigned xo = 0, wo = 0;
  ld(wa, xa, xo, wo, 0);
#pragma unroll 1
  for (int s = 0; s < NK; s++) {
    ld(wb, xb, xo, wo, 1); mm(wa, xa); sched();
    TSFC(15 + s);
    wait_lgkm_bar(); sbar();
    TSFC(1 + s);
    xo = (xo + 16384 == XNS * 16384) ? 0 : xo + 16384;
    wo = (wo + 20480 == WNS * 20480) ? 0 : wo + 20480;
    ld(wa, xa, xo, wo, 0);
    mm(wb, xb); sched();
  }
}

#define STOP 99

__global__ __launch_bounds__(512, 2) void k_all(const u16* __restrict__ x, const u16* __restrict__ wqkv, const u16* __restrict__ wo,
                                                 const u16* __restrict__ w1, const u16* __restrict__ b1, const u16* __restrict__ w2,
                                                 const u16* __restrict__ b2, u16* __restrict__ out, char* __restrict__ ws) {
  constexpr unsigned lb = 0;
  const int tid = threadIdx.x, l = tid & 63, w = __builtin_amdgcn_readfirstlane(tid >> 6);
  const int bid = blockIdx.x, xcd = bid & 7, jj = bid >> 3;
  const int rbA = 2 * xcd + (jj >> 4), ctA = jj & 15, rbB = 4 * xcd + (jj >> 3), ctB = jj & 7;
  const int g = l >> 4, i = l & 15, h = l >> 5, a31 = l & 31;
  const rsrc_t rs = __builtin_amdgcn_make_buffer_rsrc(ws, 0, 0x7fffffff, 0x00020000);
  TS(0);
  u64 E = __hip_atomic_load((const GAS u64*)ws, __ATOMIC_RELAXED, __HIP_MEMORY_SCOPE_AGENT) + 1;
  u32x2 res[5];
  unsigned biasv = 0;
  if (w < 4) {
    const u16* p = x + (size_t)(rbB * 64 + (w & 1) * 32 + (w >> 1) * 16 + i) * 640 + ctB * 80 + g * 4;
#pragma unroll
    for (int m = 0; m < 5; m++) res[m] = *(const u32x2*)(p + m * 16);
  } else {
#pragma unroll
    for (int m = 0; m < 5; m++) res[m] = (u32x2){0u, 0u};
    if (w == 4 && l < 48) biasv = *(const unsigned*)(b1 + ctA * 96 + l * 2);
    if (w == 5 && l < 40) biasv = *(const unsigned*)(b2 + ctB * 80 + l * 2);
  }
  const int q96 = (rbA - 10) * 16 + ctA;
  const int ntile = rbA < 10 ? 2 : (q96 < 80 ? 3 : 2);
  const int npiece = rbA < 10 ? 27 : ntile * 10;
  for (int p = w; p < npiece; p += 8) {
    if (rbA < 10) {
      if (p < 15) {
        const int o = p * 1024 + l * 16, row = o / 240, wi = o - row * 240, seg = wi / 80;
        dma16((rbA * 64 + row) * 3840 + seg * 1280 + ctA * 80 + (wi - seg * 80), wqkv, lb + p * 1024);
      } else {
        const int o = (p - 15) * 1024 + l * 16, row = o / 192, wi = o - row * 192;
        dma16((rbA * 64 + row) * 3072 + ctA * 192 + wi, w1, lb + p * 1024);
      }
    } else {
      const int tl = p / 10, t = q96 + 96 * tl;
      const int o = (p - tl * 10) * 1024 + l * 16, row = o / 160, wi = o - row * 160;
      if (t < 80) {
        const int c8 = t / 10, s = t - c8 * 10;
        const int pp = s * 64 + row, hp = pp / 40, r = pp - hp * 40, cc = r >> 3, e = r & 7;
        const int srow = hp * 40 + (cc < 4 ? (cc >> 1) * 20 + (cc & 1) * 8 + e : (e >> 2) * 20 + 16 + (e & 3));
        dma16(srow * 1280 + c8 * 160 + wi, wo, lb + p * 1024);
      } else {
        const int u = t - 80, c8 = u / 24, s = u - c8 * 24;
        dma16((s * 64 + row) * 1280 + c8 * 160 + wi, w2, lb + p * 1024);
      }
    }
  }
  wait_vm<0>();
  E = ((u64)__builtin_amdgcn_readfirstlane((unsigned)(E >> 32)) << 32) | (unsigned)__builtin_amdgcn_readfirstlane((unsigned)E);
  if (w == 4 && l < 48) *(LAS f32x2*)(lb + L_B1 + l * 8) = (f32x2){bflo(biasv), bfhi(biasv)};
  if (w == 5 && l < 40) *(LAS f32x2*)(lb + L_B2 + l * 8) = (f32x2){bflo(biasv), bfhi(biasv)};
  if (w == 6) *(LAS u32x2*)(lb + L_ONE + l * 8) = (u32x2){0x00003f80u, 0u};
  if (w == 7 && l < 2) *(LAS unsigned*)(lb + L_CNT + l * 4) = 0u;
  wait_lgkm_bar();
  TSC(1);
  {
    const int ntask = rbA < 10 ? 27 : ntile * 10;
    for (int tk = w; tk < ntask; tk += 8) {
      int u, pitch;
      unsigned raw, img, csp = 0;
      bool isq = false;
      if (rbA < 10) {
        if (tk < 15) { u = tk; pitch = 240; raw = 0; img = WS_WQ + (ctA * 10 + rbA) * 15360; csp = WS_CSQ + (ctA * 10 + rbA) * 512; isq = true; }
        else { u = tk - 15; pitch = 192; raw = 15360; img = WS_W1 + (ctA * 10 + rbA) * 12288; csp = WS_CS1 + (ctA * 10 + rbA) * 512; }
      } else {
        const int tl = tk / 10, t = q96 + 96 * tl;
        u = tk - tl * 10; pitch = 160; raw = tl * 10240;
        img = t < 80 ? WS_WO + t * 10240 : WS_W2 + (t - 80) * 10240;
      }
      const int q = i & 3, e = i >> 2, dq = 2 * u + (q & 1), kc = g + 4 * (q >> 1);
      int cb = dq * 8;
      if (isq) { const int seg = dq / 10, r = dq - seg * 10, sq = r < 4 ? r : (r < 8 ? r + 1 : (r == 8 ? 4 : 9)); cb = seg * 80 + sq * 8; }
      const unsigned ad = lb + raw + (kc * 8 + e) * pitch + cb;
      const bf16x8 v = cat(lds_rdtr(ad), lds_rdtr(ad + 4 * pitch));
      const int row = 8 * u + (i & 7), ch = g + 4 * (i >> 3);
      st16(rs, row * 128 + ((ch ^ ((row >> 1) & 7)) << 4), img, __builtin_bit_cast(u32x4, v));
      if (csp) {
        const u32x4 uv = __builtin_bit_cast(u32x4, v);
        float s = ((bflo(uv[0]) + bfhi(uv[0])) + (bflo(uv[1]) + bfhi(uv[1]))) + ((bflo(uv[2]) + bfhi(uv[2])) + (bflo(uv[3]) + bfhi(uv[3])));
        s += __shfl_xor(s, 8);
        s = swap32_add(swap16_add(s));
        if (l < 8) st4(rs, (8 * u + l) * 4, csp, __float_as_uint(s));
      }
    }
  }
  auto put_stats = [&](const u32x2(&r)[5], int stbuf) {
    float v[5][4], sum = 0.f;
#pragma unroll
    for (int m = 0; m < 5; m++) {
      v[m][0] = bflo(r[m][0]); v[m][1] = bfhi(r[m][0]); v[m][2] = bflo(r[m][1]); v[m][3] = bfhi(r[m][1]);
      sum += (v[m][0] + v[m][1]) + (v[m][2] + v[m][3]);
    }
    sum = swap32_add(swap16_add(sum));
    const float mb = sum * (1.0f / 80.f);
    float m2 = 0.f;
#pragma unroll
    for (int m = 0; m < 5; m++)
#pragma unroll
      for (int e = 0; e < 4; e++) { const float d = v[m][e] - mb; m2 += d * d; }
    m2 = swap32_add(swap16_add(m2));
    if (g == 0) st8(rs, ((w & 1) * 32 + (w >> 1) * 16 + i) * 8, WS_ST + stbuf * 131072 + (rbB * 8 + ctB) * 512, (u32x2){__float_as_uint(mb), __float_as_uint(m2)});
  };
  auto store_res = [&](unsigned xdst) {
    const int tok = rbB * 64 + (w & 1) * 32 + (w >> 1) * 16 + i, ch = (g & 1) * 2 + (g >> 1);
    u32x4 c[3];
#pragma unroll
    for (int p = 0; p < 3; p++) {
      unsigned a0 = res[2 * p][0], a1 = res[2 * p][1], b0 = res[p < 2 ? 2 * p + 1 : 4][0], b1_ = res[p < 2 ? 2 * p + 1 : 4][1];
      sw16(a0, b0); sw16(a1, b1_);
      c[p] = (u32x4){a0, a1, b0, b1_};
    }
    const unsigned vo = (tok & 31) * 16, so = xdst + ((tok >> 5) * 80 + ctB * 10) * 512;
    const unsigned sou = __builtin_amdgcn_readfirstlane(so);
    st16(rs, vo + ch * 512, sou, c[0]);
    st16(rs, vo + (4 + ch) * 512, sou, c[1]);
    if ((g & 1) == 0) st16(rs, vo + (8 + (g >> 1)) * 512, sou, c[2]);
  };
  if (w < 4) { store_res(WS_X2); put_stats(res, 0); }
  publish<8>(ws, flag_off(0, bid), lb + L_CNT, E, l);
  TSC(2);
  const int cl_flag = xcd + 8 * ((jj >> 4) * 16 + i);
  if (w < 4) {
    poll(ws, flag_off(0, (i >> 1) + 8 * ((i & 1) * 16 + ctA)), l < 10, E);
    const int f = (w & 1) * 64 + l;
    const unsigned src = (w < 2 ? WS_CSQ : WS_CS1) + ctA * 5120;
    float s = 0.f;
    if (f < (w < 2 ? 120 : 96)) {
#pragma unroll
      for (int k = 0; k < 10; k++) s += __uint_as_float(ld4(rs, f * 4 + k * 512, src));
    }
    *(LAS float*)(lb + (w < 2 ? L_CSQ : L_CS1) + f * 4) = s;
  } else if (w >= 6) {
    for (int r = 0; r < 4; r++) poll(ws, flag_off(0, r * 64 + l), true, E);
    if (bid == 0 && w == 6 && l == 0) __hip_atomic_store((GAS u64*)ws, E, __ATOMIC_RELAXED, __HIP_MEMORY_SCOPE_AGENT);
    wprefill<15360, 6, 10>(ws + WS_WQ + ctA * 153600 + (w - 6) * 7680, lb + 65536 + (w - 6) * 7680, l);
  }
  TSC(3);

#pragma unroll 1
  for (int layer = 0; layer < 2; layer++) {
    const int eb = 1 + 5 * layer;
    [[maybe_unused]] const int tb = 4 + layer * 24;
    if (w < 4) {
      const int wf = w & 1, wt = w >> 1;
      f32x16 acc[2][2];
#pragma unroll
      for (int a = 0; a < 2; a++)
#pragma unroll
        for (int b = 0; b < 2; b++)
#pragma unroll
          for (int r = 0; r < 16; r++) acc[a][b][r] = 0.f;
      const int frow = wf * 64 + a31;
      gemm32<2, 2, 4, 15360, 6, 10>(acc, lb + wt * 8192 + h * 512 + a31 * 16, lb + 65536 + frow * 128 + ((h ^ ((frow >> 1) & 7)) << 4) FPASS(0));
      TSB(1);
      f32x2 ms[2];
#pragma unroll
      for (int b = 0; b < 2; b++) ms[b] = *(const LAS f32x2*)(lb + L_MS + (wt * 64 + b * 32 + a31) * 8);
      const unsigned kvs = WS_KV + (layer * 16 + ctA) * 2 * 163840 + (4 * rbA + 2 * wt) * 2560 - 163840;
#pragma unroll
      for (int aa = 0; aa < 2; aa++) {
        const int a = 1 - aa;
        f32x4 cs[4];
#pragma unroll
        for (int q = 0; q < 4; q++) cs[q] = lds_rdf4(lb + L_CSQ + (wf * 64 + a * 32 + q * 8 + h * 4) * 4);
#pragma unroll
        for (int b = 0; b < 2; b++) {
          unsigned pk[8];
#pragma unroll
          for (int q = 0; q < 4; q++) {
            const float sc = (wf == 0 && (a == 0 || q == 0)) ? QSCALE : 1.0f;
            const float r1 = ms[b][1] * sc, mr = -ms[b][0];
            f32x2 v0 = {acc[a][b][4 * q + 0], acc[a][b][4 * q + 1]}, v1 = {acc[a][b][4 * q + 2], acc[a][b][4 * q + 3]};
            v0 = (v0 + (f32x2){cs[q][0], cs[q][1]} * mr) * r1;
            v1 = (v1 + (f32x2){cs[q][2], cs[q][3]} * mr) * r1;
            pk[2 * q] = cvtpk(v0[0], v0[1]); pk[2 * q + 1] = cvtpk(v1[0], v1[1]);
          }
#pragma unroll
          for (int pr = 0; pr < 2; pr++) {
            sw32(pk[4 * pr], pk[4 * pr + 2]); sw32(pk[4 * pr + 1], pk[4 * pr + 3]);
            const u32x4 v = {pk[4 * pr], pk[4 * pr + 1], pk[4 * pr + 2], pk[4 * pr + 3]};
            const int ci = 8 * wf + 4 * a + 2 * pr + h, which = (ci >= 5) + (ci >= 10) + (ci >= 15), c = ci - 5 * which;
            const int pos = a31 ^ (which == 2 ? (c == 4 ? 8 : (c & 1) * 4) : 0);
            const unsigned off = c * 512 + pos * 16;
            if (which == 1 || which == 2) st16(rs, off + which * 163840 + b * 2560, kvs, v);
            if (which < 3) *(LAS u32x4*)(lb + (which == 0 ? L_Q : (which == 1 ? L_K + 20480 : L_V + 20480)) + off + (2 * wt + b) * 2560) = v;
          }
        }
      }
      wait_lgkm_bar();
    } else if (w < 6) {
      const int lw = w - 4;
      const char* gx = ws + WS_X2 + (size_t)(4 * rbA + 2 * lw) * 80 * 512;
      poll(ws, flag_off(layer == 0 ? 0 : 5, cl_flag), l < 16, E);
      TSB(0);
      xloader<4, 10, true>(80 * 512, gx, 4096u, lb + lw * 8192, ws + WS_ST + (2 * layer) * 131072 + (2 * rbA + lw) * 4096, lb + L_MS + (lw * 64 + l) * 8, l FPASS(0));
      bar();
    } else {
      wloop<15360, 6, 10>(ws + WS_WQ + ctA * 153600 + (w - 6) * 7680, lb + 65536 + (w - 6) * 7680, l FPASS(0));
      bar();
      wprefill<20480, 4, 5>(ws + WS_WO + ctB * 102400 + (w - 6) * 10240, lb + 73728 + (w - 6) * 10240, l);
    }
    TSB(2);
    if (STOP == 1) break;
    {
      const int hd = w & 1, qt = w < 2 ? 3 : (w < 4 ? 2 : (w < 6 ? 0 : 1));
      const int gmin = rbA == 0 ? 8 : (rbA == 1 ? 4 : 0);
      if (w < 4) publish<4>(ws, flag_off(eb + 0, bid), lb + L_CNT + 4, E, l);
      TSB(3);
      const unsigned qa = lb + L_Q + qt * 2560 + a31 * 16;
      const bf16x8 q0f = lds_rd128(qa + (2 * hd + h) * 512);
      const s16x4 qt4 = lds_rd64(qa + 2048 + hd * 8);
      u32x4 q1u;
      { const u32x2 t = __builtin_bit_cast(u32x2, qt4); q1u = (u32x4){h ? 0u : t[0], h ? 0u : t[1], 0u, 0u}; }
      const unsigned ka0 = lb + L_K + (2 * hd + h) * 512 + a31 * 16, ka1 = lb + L_K + 2048 + a31 * 16 + hd * 8;
      const bool cst = (g & 1) && (i & 3) != 0;
      const unsigned vstr = cst ? 0u : 2560u;
      unsigned vaA, vaB;
      {
        const int k = 4 * h + (i >> 2);
        if (g & 1) {
          vaA = lb + L_V + 2048 + ((k ^ 8) << 4) + hd * 8; vaB = lb + L_V + 2048 + (((k + 8) ^ 8) << 4) + hd * 8;
          if (cst) { vaA = lb + L_ONE + (h ? 128 : 192); vaB = lb + L_ONE + (h ? 0 : 64); }
        } else {
          const int odd = (i >> 1) & 1;
          vaA = lb + L_V + (2 * hd + odd) * 512 + ((k ^ (odd * 4)) << 4) + (i & 1) * 8; vaB = vaA + 128;
        }
      }
      auto qk = [&](int G) {
        const bf16x8 k0 = lds_rd128(ka0 + G * 2560);
        const u32x2 t = __builtin_bit_cast(u32x2, lds_rd64(ka1 + G * 2560));
        const u32x4 k1 = {t[0], t[1], 0x3f803f80u, 0x00003f80u};
        f32x16 s;
#pragma unroll
        for (int r = 0; r < 16; r++) s[r] = 0.f;
        s = __builtin_amdgcn_mfma_f32_32x32x16_bf16(k0, q0f, s, 0, 0, 0);
        s = __builtin_amdgcn_mfma_f32_32x32x16_bf16(__builtin_bit_cast(bf16x8, k1), __builtin_bit_cast(bf16x8, q1u), s, 0, 0, 0);
        return s;
      };
      const float NEG = -__builtin_inff();
      auto maskit = [&](f32x16& s, int mode) {
        if (mode == 0) return;
        const int d = a31 - 4 * h;
#pragma unroll
        for (int r = 0; r < 16; r++) { const int b = (r & 3) + 8 * (r >> 2); const bool le = b <= d; s[r] = (le == (mode == 1)) ? s[r] : NEG; }
      };
      f32x16 o;
      auto pv = [&](const f32x16& s, int G) {
        unsigned pk[8];
#pragma unroll
        for (int r = 0; r < 8; r++) pk[r] = cvtpk(__builtin_amdgcn_exp2f(s[2 * r]), __builtin_amdgcn_exp2f(s[2 * r + 1]));
        const unsigned pA = vaA + G * vstr, pB = vaB + G * vstr;
        const bf16x8 v0 = cat(lds_rdtr(pA), lds_rdtr(pB)), v1 = cat(lds_rdtr(pA + 256), lds_rdtr(pB + 256));
        const u32x4 p0 = {pk[0], pk[1], pk[2], pk[3]}, p1 = {pk[4], pk[5], pk[6], pk[7]};
        o = __builtin_amdgcn_mfma_f32_32x32x16_bf16(v0, __builtin_bit_cast(bf16x8, p0), o, 0, 0, 0);
        o = __builtin_amdgcn_mfma_f32_32x32x16_bf16(v1, __builtin_bit_cast(bf16x8, p1), o, 0, 0, 0);
      };
      auto rowmax = [&](const f32x16& s) {
        float m = s[0];
#pragma unroll
        for (int r = 1; r < 16; r++) m = fmaxf(m, s[r]);
        return m;
      };
      auto setm = [&](float m) {
        const float nm = -m;
        const unsigned hi = __float_as_uint(nm) & 0xffff0000u;
        const float r1 = nm - __uint_as_float(hi);
        const unsigned mid = __float_as_uint(r1) & 0xffff0000u;
        const float r2 = r1 - __uint_as_float(mid);
        const unsigned lo = __float_as_uint(r2) & 0xffff0000u;
        q1u[2] = h ? 0u : ((hi >> 16) | mid);
        q1u[3] = h ? 0u : (lo >> 16);
      };
      {
        f32x16 s = qk(qt + 8);
        maskit(s, 1);
        const float m = swap32_max(rowmax(s));
        setm(m);
#pragma unroll
        for (int r = 0; r < 16; r++) { o[r] = 0.f; s[r] -= m; }
        pv(s, qt + 8);
      }
#pragma unroll 1
      for (int G = qt + 7; G >= 8; G--) { f32x16 s = qk(G); pv(s, G); }
      TSB(4);
      if (w == 4 || w == 5) {
        if (gmin < 8) {
          const int rr = rbA - 1 - (l & 1);
          poll(ws, flag_off(eb + 0, (rr >> 1) + 8 * ((rr & 1) * 16 + ctA)), l < 2 && rr >= 0, E);
          const char* src = ws + WS_KV + (size_t)((layer * 16 + ctA) * 2 + (w - 4)) * 163840 + (ptrdiff_t)(4 * rbA - 8) * 2560;
          const unsigned dst = lb + (w == 4 ? L_K : L_V);
          for (int p = gmin == 4 ? 10 : 0; p < 20; p++) dma16(l * 16 + p * 1024, src, dst + p * 1024);
          wait_vm<0>();
        }
      }
      TSB(5);
      bar();
      TSB(6);
      {
        const int glo = gmin > qt + 1 ? gmin : qt + 1;
#pragma unroll 1
        for (int G = 7; G >= glo; G--) { f32x16 s = qk(G); pv(s, G); }
        if (gmin == 0) { f32x16 s = qk(qt); maskit(s, 2); pv(s, qt); }
      }
      float lsum;
      { auto r = __builtin_amdgcn_permlane32_swap(__float_as_uint(o[8]), __float_as_uint(o[8]), false, false); lsum = __uint_as_float(r[1]); }
      if (__builtin_amdgcn_ballot_w64(!(lsum <= 1.0995116e12f && lsum >= 0.25f)) != 0) {
        q1u[2] = 0u; q1u[3] = 0u;
        const int g0 = gmin > qt ? gmin : qt;
        float m = NEG;
#pragma unroll 1
        for (int G = g0; G <= qt + 8; G++) { f32x16 s = qk(G); maskit(s, G == qt + 8 ? 1 : (G == qt ? 2 : 0)); m = fmaxf(m, rowmax(s)); }
        m = swap32_max(m);
#pragma unroll
        for (int r = 0; r < 16; r++) o[r] = 0.f;
#pragma unroll 1
        for (int G = g0; G <= qt + 8; G++) {
          f32x16 s = qk(G);
          maskit(s, G == qt + 8 ? 1 : (G == qt ? 2 : 0));
#pragma unroll
          for (int r = 0; r < 16; r++) s[r] -= m;
          pv(s, G);
        }
        auto r = __builtin_amdgcn_permlane32_swap(__float_as_uint(o[8]), __float_as_uint(o[8]), false, false);
        lsum = __uint_as_float(r[1]);
      }
      const float inv = 1.0f / lsum;
      unsigned pk[6];
#pragma unroll
      for (int r = 0; r < 6; r++) pk[r] = cvtpk(o[2 * r] * inv, o[2 * r + 1] * inv);
      sw32(pk[0], pk[2]); sw32(pk[1], pk[3]);
      const unsigned oa = lb + L_Q + qt * 2560 + a31 * 16;
      *(LAS u32x4*)(oa + (2 * hd + h) * 512) = (u32x4){pk[0], pk[1], pk[2], pk[3]};
      if (h == 0) *(LAS u32x2*)(oa + 2048 + hd * 8) = (u32x2){pk[4], pk[5]};
      TSB(7);
      wait_lgkm_bar();
      if (w < 4) {
        const unsigned so = WS_O + layer * 0x280000 + ((4 * rbA + w) * 80 + 5 * ctA) * 512;
        const unsigned la = lb + L_Q + w * 2560 + l * 16;
        const u32x4 v0 = lds_rdu4(la), v1 = lds_rdu4(la + 1024);
        st16(rs, l * 16, so, v0);
        st16(rs, l * 16 + 1024, so, v1);
        if (l < 32) st16(rs, l * 16 + 2048, so, lds_rdu4(la + 2048));
        publish<4>(ws, flag_off(eb + 1, bid), lb + L_CNT + 4, E, l);
      }
      TSB(8);
    }
    if (STOP == 2) break;
    auto res_epilogue = [&](f32x4(&acc0)[5], f32x4(&acc1)[5], bool bias, bool last, unsigned xdst, int stbuf, int edge) {
      const int wt = w & 1, wk = w >> 1;
#pragma unroll
      for (int m = 0; m < 5; m++) *(LAS f32x4*)(lb + ((wt * 2 + wk) * 5 + m) * 1024 + l * 16) = acc1[m];
      wait_lgkm_bar();
#pragma unroll
      for (int m = 0; m < 5; m++) {
        const f32x4 ot = lds_rdf4(lb + ((wt * 2 + (1 - wk)) * 5 + m) * 1024 + l * 16);
        f32x4 y = acc0[m] + ot + (f32x4){bflo(res[m][0]), bfhi(res[m][0]), bflo(res[m][1]), bfhi(res[m][1])};
        if (bias) y += lds_rdf4(lb + L_B2 + (m * 16 + g * 4) * 4);
        res[m] = (u32x2){cvtpk(y[0], y[1]), cvtpk(y[2], y[3])};
      }
      const int tok = rbB * 64 + wt * 32 + wk * 16 + i, ch = (g & 1) * 2 + (g >> 1);
      u32x4 c[3];
#pragma unroll
      for (int p = 0; p < 3; p++) {
        unsigned a0 = res[2 * p][0], a1 = res[2 * p][1], b0 = res[p < 2 ? 2 * p + 1 : 4][0], b1_ = res[p < 2 ? 2 * p + 1 : 4][1];
        sw16(a0, b0); sw16(a1, b1_);
        c[p] = (u32x4){a0, a1, b0, b1_};
      }
      if (!last) {
        const unsigned vo = (tok & 31) * 16, so = xdst + ((tok >> 5) * 80 + ctB * 10) * 512;
        const unsigned sou = __builtin_amdgcn_readfirstlane(so);
        st16(rs, vo + ch * 512, sou, c[0]);
        st16(rs, vo + (4 + ch) * 512, sou, c[1]);
        if ((g & 1) == 0) st16(rs, vo + (8 + (g >> 1)) * 512, sou, c[2]);
        put_stats(res, stbuf);
        publish<4>(ws, flag_off(edge, bid), lb + L_CNT + 4, E, l);
      } else {
        char* op = (char*)out + (size_t)tok * 1280 + ctB * 160;
        *(u32x4*)(op + ch * 16) = c[0];
        *(u32x4*)(op + (4 + ch) * 16) = c[1];
        if ((g & 1) == 0) *(u32x4*)(op + (8 + (g >> 1)) * 16) = c[2];
      }
    };
    if (w < 4) {
      const int wt = w & 1, wk = w >> 1;
      f32x4 acc0[5], acc1[5];
#pragma unroll
      for (int m = 0; m < 5; m++) { acc0[m] = (f32x4){0, 0, 0, 0}; acc1[m] = (f32x4){0, 0, 0, 0}; }
      const unsigned xb = lb + wk * 8192 + wt * 4096 + g * 512 + i * 16;
      const unsigned sw = (g ^ ((i >> 1) & 7)) << 4;
      gemm16<4, 4, 5>(acc0, acc1, xb + wk * 256, xb + (1 - wk) * 256, lb + 73728 + wk * 10240 + i * 128 + sw FPASS(1));
      TSB(10);
      res_epilogue(acc0, acc1, false, false, WS_X1 + layer * 0x280000, 2 * layer + 1, eb + 2);
    } else if (w < 6) {
      const int lw = w - 4;
      poll(ws, flag_off(eb + 1, cl_flag), l < 16, E);
      TSB(9);
      xloader<4, 5, false>(80 * 512, ws + WS_O + layer * 0x280000 + (size_t)(2 * rbB) * 80 * 512 + lw * 4096, 8192u, lb + lw * 8192, nullptr, 0, l FPASS(1));
      bar();
    } else {
      wloop<20480, 4, 5>(ws + WS_WO + ctB * 102400 + (w - 6) * 10240, lb + 73728 + (w - 6) * 10240, l FPASS(1));
      bar();
      wprefill<12288, 7, 10>(ws + WS_W1 + ctA * 122880 + (w - 6) * 6144, lb + 71680 + (w - 6) * 6144, l);
    }
    TSB(11);
    if (STOP == 3) break;
    if (w < 4) {
      f32x16 acc[3][1];
#pragma unroll
      for (int a = 0; a < 3; a++)
#pragma unroll
        for (int r = 0; r < 16; r++) acc[a][0][r] = 0.f;
      const int trow = w * 32 + a31;
      gemm32<3, 1, 4, 12288, 7, 10>(acc, lb + w * 4096 + h * 512 + a31 * 16, lb + 71680 + a31 * 128 + ((h ^ ((a31 >> 1) & 7)) << 4) FPASS(2));
      TSB(13);
      const f32x2 ms = *(const LAS f32x2*)(lb + L_MS + trow * 8);
      const float rstd = ms[1], mr = -ms[0] * ms[1];
      const unsigned so = WS_H + layer * 0x600000 + ((4 * rbA + w) * 192 + 12 * ctA) * 512;
#pragma unroll
      for (int a = 0; a < 3; a++) {
        unsigned pk[8];
#pragma unroll
        for (int q = 0; q < 4; q++) {
          const f32x4 cs = lds_rdf4(lb + L_CS1 + (a * 32 + q * 8 + h * 4) * 4), bb = lds_rdf4(lb + L_B1 + (a * 32 + q * 8 + h * 4) * 4);
          f32x2 t0 = (f32x2){cs[0], cs[1]} * mr + (f32x2){bb[0], bb[1]}, t1 = (f32x2){cs[2], cs[3]} * mr + (f32x2){bb[2], bb[3]};
          f32x2 v0 = (f32x2){acc[a][0][4 * q + 0], acc[a][0][4 * q + 1]} * rstd + t0, v1 = (f32x2){acc[a][0][4 * q + 2], acc[a][0][4 * q + 3]} * rstd + t1;
          v0 = (f32x2){fmaxf(v0[0], 0.f), fmaxf(v0[1], 0.f)}; v1 = (f32x2){fmaxf(v1[0], 0.f), fmaxf(v1[1], 0.f)};
          v0 *= v0; v1 *= v1;
          pk[2 * q] = cvtpk(v0[0], v0[1]); pk[2 * q + 1] = cvtpk(v1[0], v1[1]);
        }
#pragma unroll
        for (int pr = 0; pr < 2; pr++) {
          sw32(pk[4 * pr], pk[4 * pr + 2]); sw32(pk[4 * pr + 1], pk[4 * pr + 3]);
          st16(rs, (4 * a + 2 * pr + h) * 512 + a31 * 16, so, (u32x4){pk[4 * pr], pk[4 * pr + 1], pk[4 * pr + 2], pk[4 * pr + 3]});
        }
      }
      publish<4>(ws, flag_off(eb + 3, bid), lb + L_CNT + 4, E, l);
    } else if (w < 6) {
      const int lw = w - 4;
      poll(ws, flag_off(eb + 2, cl_flag), l < 16, E);
      TSB(12);
      xloader<4, 10, true>(80 * 512, ws + WS_X1 + layer * 0x280000 + (size_t)(4 * rbA + 2 * lw) * 80 * 512, 4096u, lb + lw * 8192,
                           ws + WS_ST + (2 * layer + 1) * 131072 + (2 * rbA + lw) * 4096, lb + L_MS + (lw * 64 + l) * 8, l FPASS(2));
    } else {
      wloop<12288, 7, 10>(ws + WS_W1 + ctA * 122880 + (w - 6) * 6144, lb + 71680 + (w - 6) * 6144, l FPASS(2));
      wprefill<20480, 5, 12>(ws + WS_W2 + ctB * 245760 + (w - 6) * 10240, lb + 49152 + (w - 6) * 10240, l);
    }
    TSB(14);
    if (STOP == 4) break;
    if (w < 4) {
      const int wt = w & 1, wk = w >> 1;
      f32x4 acc0[5], acc1[5];
#pragma unroll
      for (int m = 0; m < 5; m++) { acc0[m] = (f32x4){0, 0, 0, 0}; acc1[m] = (f32x4){0, 0, 0, 0}; }
      const unsigned xb = lb + wk * 8192 + wt * 4096 + g * 512 + i * 16;
      const unsigned sw = (g ^ ((i >> 1) & 7)) << 4;
      gemm16<3, 5, 12>(acc0, acc1, xb + wk * 256, xb + (1 - wk) * 256, lb + 49152 + wk * 10240 + i * 128 + sw FPASS(3));
      TSB(16);
      res_epilogue(acc0, acc1, true, layer == 1, WS_X2, 2, eb + 4);
    } else if (w < 6) {
      const int lw = w - 4;
      poll(ws, flag_off(eb + 3, cl_flag), l < 16, E);
      TSB(15);
      xloader<3, 12, false>(192 * 512, ws + WS_H + layer * 0x600000 + (size_t)(2 * rbB) * 192 * 512 + lw * 4096, 8192u, lb + lw * 8192, nullptr, 0, l FPASS(3));
      bar();
    } else {
      wloop<20480, 5, 12>(ws + WS_W2 + ctB * 245760 + (w - 6) * 10240, lb + 49152 + (w - 6) * 10240, l FPASS(3));
      bar();
      if (layer == 0) wprefill<15360, 6, 10>(ws + WS_WQ + ctA * 153600 + (w - 6) * 7680, lb + 65536 + (w - 6) * 7680, l);
    }
    TSB(17);
    if (STOP == 5) break;
  }
}

extern "C" void kernel_launch(void* const* d_in, const int* in_sizes, int n_in, void* d_out, int out_size, void* d_ws, size_t ws_size,
                              hipStream_t stream) {
  hipFuncSetAttribute((const void*)k_all, hipFuncAttributeMaxDynamicSharedMemorySize, LDS_BYTES);
  k_all<<<256, 512, LDS_BYTES, stream>>>((const u16*)d_in[0], (const u16*)d_in[1], (const u16*)d_in[2], (const u16*)d_in[4], (const u16*)d_in[5],
                                         (const u16*)d_in[6], (const u16*)d_in[7], (u16*)d_out, (char*)d_ws);
}
```

```cpp
#include <hip/hip_runtime.h>
#include <stdint.h>
#include <type_traits>
#include <utility>

typedef short bf16x8 __attribute__((ext_vector_type(8)));
typedef short s16x4 __attribute__((ext_vector_type(4)));
typedef float f32x2 __attribute__((ext_vector_type(2)));
typedef float f32x4 __attribute__((ext_vector_type(4)));
typedef float f32x16 __attribute__((ext_vector_type(16)));
typedef unsigned u32x2 __attribute__((ext_vector_type(2)));
typedef unsigned u32x4 __attribute__((ext_vector_type(4)));
typedef __bf16 bf16x2_t __attribute__((ext_vector_type(2)));
typedef unsigned short u16;
typedef unsigned long long u64;
#define LAS __attribute__((address_space(3)))
#define GAS __attribute__((address_space(1)))
#define DI __device__ __forceinline__
template <int V> using IC = std::integral_constant<int, V>;

constexpr float LN_EPS = 1e-5f;
constexpr float QSCALE = 0.22360679774997896f * 1.4426950408889634f;

#define FSTR 32
constexpr unsigned WS_FE = 0x1000, FE_STR = 256 * FSTR;
constexpr unsigned WS_P1W = 0x58000, WS_P1X = 0x58800, WS_P2 = 0x59000, WS_P1C = 0x59800, WS_PX4 = 0x5A000, WS_PW4 = 0x5C000;
constexpr unsigned WS_ST = 0x60000;
constexpr unsigned WS_CSQ = 0xE0000;
constexpr unsigned WS_CS1 = 0x100000;
constexpr unsigned WS_WQ = 0x120000;
constexpr unsigned WS_W1 = 0x378000;
constexpr unsigned WS_WO = 0x558000;
constexpr unsigned WS_W2 = 0x620000;
constexpr unsigned WS_KV = 0x800000;
constexpr unsigned WS_O = 0x1200000;
constexpr unsigned WS_X1 = 0x1700000;
constexpr unsigned WS_X2 = 0x1C00000;
constexpr unsigned WS_H = 0x2100000;
constexpr unsigned WS_DBG = 0x3000000;
constexpr unsigned L_K = 0, L_V = 30720, L_Q = 61440;
constexpr unsigned L_CSQ = 157696, L_CS1 = 158208, L_B1 = 158720, L_B2 = 159232, L_MS = 159744, L_CNT = 160768, L_ONE = 161024, L_DUMMY = 0;
constexpr unsigned L_PFD = 161536;
constexpr unsigned L_RQ = 0, L_PQ = 10240, L_R1 = 32768, L_R2 = 40960, L_RO = 49152, L_P1 = 57344;
constexpr unsigned LDS_BYTES = 163840;
#define SPINMAX 400000
constexpr int SPIN_MAX = SPINMAX;

template <int I, int N, class Fn> DI void static_for(Fn&& f) { if constexpr (I < N) { f(IC<I>{}); static_for<I + 1, N>(f); } }
DI unsigned cvtpk(float lo, float hi) { f32x2 v = {lo, hi}; bf16x2_t b = __builtin_convertvector(v, bf16x2_t); return __builtin_bit_cast(unsigned, b); }
DI float bflo(unsigned u) { return __uint_as_float(u << 16); }
DI float bfhi(unsigned u) { return __uint_as_float(u & 0xffff0000u); }
DI void dma16(unsigned voff, const void* sbase, unsigned lds) {
  asm volatile("s_mov_b32 m0, %2\n\ts_nop 0\n\tglobal_load_lds_dwordx4 %0, %1 sc1" ::"v"(voff), "s"(sbase), "s"(lds) : "memory");
}
template <int IMM> DI void dma4(unsigned v0, const void* sbase, unsigned ldsb) {
  asm volatile("s_add_u32 m0, %2, %3\n\ts_nop 0\n\tglobal_load_lds_dwordx4 %0, %1 sc1\n\tglobal_load_lds_dwordx4 %0, %1 offset:1024 sc1\n\t"
               "global_load_lds_dwordx4 %0, %1 offset:2048 sc1\n\tglobal_load_lds_dwordx4 %0, %1 offset:3072 sc1"
               ::"v"(v0), "s"(sbase), "s"(ldsb), "n"(IMM) : "memory", "scc");
}
template <int IMM> DI void dma2(unsigned v0, const void* sbase, unsigned ldsb) {
  asm volatile("s_add_u32 m0, %2, %3\n\ts_nop 0\n\tglobal_load_lds_dwordx4 %0, %1 sc1\n\tglobal_load_lds_dwordx4 %0, %1 offset:1024 sc1"
               ::"v"(v0), "s"(sbase), "s"(ldsb), "n"(IMM) : "memory", "scc");
}
template <int IMM> DI void dma3(unsigned v0, const void* sbase, unsigned ldsb) {
  asm volatile("s_add_u32 m0, %2, %3\n\ts_nop 0\n\tglobal_load_lds_dwordx4 %0, %1 sc1\n\tglobal_load_lds_dwordx4 %0, %1 offset:1024 sc1\n\t"
               "global_load_lds_dwordx4 %0, %1 offset:2048 sc1"
               ::"v"(v0), "s"(sbase), "s"(ldsb), "n"(IMM) : "memory", "scc");
}
template <int IMM, int OFF> DI void dma1(unsigned v0, const void* sbase, unsigned ldsb) {
  asm volatile("s_add_u32 m0, %2, %3\n\ts_nop 0\n\tglobal_load_lds_dwordx4 %0, %1 offset:%4 sc1"
               ::"v"(v0), "s"(sbase), "s"(ldsb), "n"(IMM), "n"(OFF) : "memory", "scc");
}
DI void dmab(unsigned v0, unsigned v1, const void* sbase, unsigned lds) {
  asm volatile("s_mov_b32 m0, %3\n\ts_nop 0\n\tglobal_load_lds_dwordx4 %0, %2 sc1\n\ts_mov_b32 exec_hi, 0xffffff\n\t"
               "global_load_lds_dwordx4 %1, %2 offset:1024 sc1\n\ts_mov_b32 exec_hi, -1"
               ::"v"(v0), "v"(v1), "s"(sbase), "s"(lds) : "memory");
}
DI float dot8(u32x4 v, float s) {
#pragma unroll
  for (int e = 0; e < 4; e++) { const unsigned t = v[e]; s = __builtin_amdgcn_fdot2_f32_bf16(__builtin_bit_cast(bf16x2_t, t), __builtin_bit_cast(bf16x2_t, 0x3f803f80u), s, false); }
  return s;
}
DI void warm1(unsigned v, const char* p) {
  asm volatile("s_mov_b32 m0, %2\n\ts_nop 0\n\tglobal_load_lds_dword %0, %1 sc1" ::"v"(v), "s"(p), "n"(L_PFD) : "memory");
}
template <int LINES> DI void l2warm(const char* g, int l) {
  constexpr int N = (LINES + 63) / 64;
  static_for<0, N>([&](auto k) {
    constexpr int last = LINES - 1 - 64 * k;
    const unsigned v = (unsigned)(l < last ? l : last) * 128u;
    warm1(v, g + k * 8192);
  });
}
template <int LINES> constexpr int warm_n() { return (LINES + 63) / 64; }
template <int OFF> DI void gld8(u32x2& d, unsigned voff, const void* sbase) {
  asm volatile("global_load_dwordx2 %0, %1, %2 offset:%3 sc1" : "=v"(d) : "v"(voff), "s"(sbase), "n"(OFF) : "memory");
}
template <int OFF> DI void gld16(u32x4& d, unsigned voff, const void* sbase) {
  asm volatile("global_load_dwordx4 %0, %1, %2 offset:%3 sc1" : "=v"(d) : "v"(voff), "s"(sbase), "n"(OFF) : "memory");
}
template <int OFF> DI void gld4(unsigned& d, unsigned voff, const void* sbase) {
  asm volatile("global_load_dword %0, %1, %2 offset:%3 sc1" : "=v"(d) : "v"(voff), "s"(sbase), "n"(OFF) : "memory");
}
template <class T> DI void keep(T& v) { asm volatile("" : "+v"(v)); }
template <class T> DI void opaque_s(T& v) { asm volatile("" : "+s"(v)); }
template <int N> DI void wait_vm() { asm volatile("s_waitcnt vmcnt(%0)" ::"n"(N) : "memory"); }
DI void wait_lgkm() { asm volatile("s_waitcnt lgkmcnt(0)" ::: "memory"); }
DI void bar() { asm volatile("s_barrier" ::: "memory"); }
DI void wait_lgkm_bar() { asm volatile("s_waitcnt lgkmcnt(0)\n\ts_barrier" ::: "memory"); }
DI void sbar() { __builtin_amdgcn_sched_barrier(0); }
DI bf16x8 lds_rd128(unsigned a) { return *(const LAS bf16x8*)a; }
DI f32x4 lds_rdf4(unsigned a) { return *(const LAS f32x4*)a; }
DI f32x2 lds_rdf2(unsigned a) { return *(const LAS f32x2*)a; }
DI u32x4 lds_rdu4(unsigned a) { return *(const LAS u32x4*)a; }
DI s16x4 lds_rd64(unsigned a) { return *(const LAS s16x4*)a; }
DI s16x4 lds_rdtr(unsigned a) { return __builtin_amdgcn_ds_read_tr16_b64_v4i16((LAS s16x4*)a); }
DI bf16x8 cat(s16x4 a, s16x4 b) { return (bf16x8){a[0], a[1], a[2], a[3], b[0], b[1], b[2], b[3]}; }
DI float swap32_add(float v) { auto r = __builtin_amdgcn_permlane32_swap(__float_as_uint(v), __float_as_uint(v), false, false); return __uint_as_float(r[0]) + __uint_as_float(r[1]); }
DI float swap32_max(float v) { auto r = __builtin_amdgcn_permlane32_swap(__float_as_uint(v), __float_as_uint(v), false, false); return fmaxf(__uint_as_float(r[0]), __uint_as_float(r[1])); }
DI float swap16_add(float v) { auto r = __builtin_amdgcn_permlane16_swap(__float_as_uint(v), __float_as_uint(v), false, false); return __uint_as_float(r[0]) + __uint_as_float(r[1]); }
typedef __amdgpu_buffer_rsrc_t rsrc_t;
DI void st16(rsrc_t r, unsigned voff, unsigned soff, u32x4 v) { __builtin_amdgcn_raw_buffer_store_b128(v, r, voff, soff, 16); }
DI void st8(rsrc_t r, unsigned voff, unsigned soff, u32x2 v) { __builtin_amdgcn_raw_buffer_store_b64(v, r, voff, soff, 16); }
DI void st4(rsrc_t r, unsigned voff, unsigned soff, unsigned v) { __builtin_amdgcn_raw_buffer_store_b32(v, r, voff, soff, 16); }
DI void sw32(unsigned& a, unsigned& b) { auto r = __builtin_amdgcn_permlane32_swap(a, b, false, false); a = r[0]; b = r[1]; }
DI void sw16(unsigned& a, unsigned& b) { auto r = __builtin_amdgcn_permlane16_swap(a, b, false, false); a = r[0]; b = r[1]; }
DI void mfma32(f32x16& c, const bf16x8& a, const bf16x8& b) { asm volatile("v_mfma_f32_32x32x16_bf16 %0, %1, %2, %0" : "+v"(c) : "v"(a), "v"(b)); }
DI void mfma16(f32x4& c, const bf16x8& a, const bf16x8& b) { asm volatile("v_mfma_f32_16x16x32_bf16 %0, %1, %2, %0" : "+v"(c) : "v"(a), "v"(b)); }
DI void mfma_drain() { asm volatile("s_nop 15\n\ts_nop 3" ::: "memory"); }

#define TS(id)
#define TARG
#define TPASS
#define FARG
#define FPASS(ph)
#define TSF(k)
#define TSB(k) TS(tb + (k))
#define TSP(k) TS(k)
#define FINEL 1

DI void poll(const char* base, unsigned off, bool active, u64 E) {
  const GAS u64* p = (const GAS u64*)(base + off);
#pragma unroll 1
  for (int it = 0; it < SPIN_MAX; it++) {
    u64 v = E;
    if (active) v = __hip_atomic_load(p, __ATOMIC_RELAXED, __HIP_MEMORY_SCOPE_AGENT);
    if (__builtin_amdgcn_ballot_w64(v != E) == 0) break;
    __builtin_amdgcn_s_sleep(1);
  }
}
DI void st_flag(char* ws, unsigned off, u64 E) { opaque_s(E); __hip_atomic_store((GAS u64*)(ws + off), E, __ATOMIC_RELAXED, __HIP_MEMORY_SCOPE_AGENT); }
DI void publish_wave(char* ws, unsigned off, u64 E, int l) {
  wait_vm<0>();
  if (l == 0) st_flag(ws, off, E);
}
template <int NW> DI bool arrive(unsigned cnt_lds, int l) {
  unsigned old = 0;
  if (l == 0) old = __hip_atomic_fetch_add((LAS unsigned*)cnt_lds, 1u, __ATOMIC_RELAXED, __HIP_MEMORY_SCOPE_WORKGROUP);
  return l == 0 && old == NW - 1;
}
#define QPRE 10
#define W1PRE 10
#define XRQ 10
#define XR1 16
#define XR2 10
#define WDELAY 3
#define SBAR 1
#define CG 1
#define KVEARLY 1
#define WOPF 1
#define CGB CG
#define PSLP 1
#define RSLP 1
#define WINO 6
#define WINQ 8
#define WIN1 8
#define WIN2 6
#define LSLP 1
#define LSLPB 1
#define WB4 0
#define PARK1 1
#define LSLPA 0
#define FSLP 0
#define NB1 18
#define PRO2 1
#define PST 0
#define PRO4 1
#define WARMT 0
#define SLE 0
#define WIF 0
#define PRIO 0
#define PRM 0
#define SETP(n) do { if (PRM) __builtin_amdgcn_s_setprio((PRM >> (n)) & 1); } while (0)
#define HPERM 1
#define DEFP 1
#define RESB 0
#define DEF1 2
#define SLW 4

template <int NM, int ND, int PT, class FM, class FR> DI void inter(FM&& fm, FR&& fr) {
  static_for<0, NM>([&](auto m) {
    fm(m); sbar();
    constexpr int q0 = m * PT, q1 = (m + 1) * PT;
    constexpr int r0 = q0 < ND ? q0 : ND, r1 = q1 < ND ? q1 : ND;
    constexpr int r1x = (m == NM - 1) ? ND : r1;
    static_for<r0, r1x>([&](auto r) { fr(r); });
    sbar();
  });
}
template <bool NOPS, class T> DI void drain1(T& a) {
  if constexpr (NOPS) asm volatile("s_nop 15\n\ts_nop 3" : "+v"(a)); else asm volatile("" : "+v"(a));
}
using I0 = IC<0>; using I1 = IC<1>; using I2 = IC<2>; using I3 = IC<3>;
template <int N, class T> DI void wait_vm_dep(T& v) { asm volatile("s_waitcnt vmcnt(%1)" : "+v"(v) : "n"(N) : "memory"); }
template <int N, class T> DI void wait_vm_dep2(T& a, T& b) { asm volatile("s_waitcnt vmcnt(%2)" : "+v"(a), "+v"(b) : "n"(N) : "memory"); }
constexpr unsigned HOLE = 0xffffffffu;
DI unsigned fillw(unsigned v) { return v < HOLE - 1 ? v : HOLE - 1; }
DI unsigned umax(unsigned a, unsigned b) { return a > b ? a : b; }
DI unsigned umax4(const u32x4& a) { return umax(umax(umax(a[0], a[1]), a[2]), a[3]); }
DI bool any_hole(unsigned m) { return __builtin_expect(__builtin_amdgcn_ballot_w64(m == HOLE) != 0, 0); }
DI bool any_hole2(unsigned m) { return any_hole(m); }
template <int OFF> DI void gld16r(u32x4& d, unsigned voff, const void* sbase) {
  asm volatile("global_load_dwordx4 %0, %1, %2 offset:%3 sc1" : "+v"(d) : "v"(voff), "s"(sbase), "n"(OFF) : "memory");
}
DI void mfma32u(f32x16& c, const bf16x8& a, const u32x4& b) { asm volatile("v_mfma_f32_32x32x16_bf16 %0, %1, %2, %0" : "+v"(c) : "v"(a), "v"(b)); }
DI void mfma16u(f32x4& c, const bf16x8& a, const u32x4& b) { asm volatile("v_mfma_f32_16x16x32_bf16 %0, %1, %2, %0" : "+v"(c) : "v"(a), "v"(b)); }
#define STR2(x) #x
#define STR(x) STR2(x)
template <int ID, int W, int OFF> DI void hole32(u64 m, unsigned xv, const char* gx, u32x4& x0) {
  asm volatile("s_cmp_lg_u64 %[m], 0\n\ts_cbranch_scc1 .Lhs%c[id]\n.Lhb%c[id]:\n\t.subsection 1\n.Lhs%c[id]:\n\ts_mov_b32 s95, " STR(SPINMAX) "\n"
    "v_add_u32 v254, %c[off], %[xv]\n.Lhp%c[id]:\n"
    ".if %c[w] > 0\nglobal_load_dwordx4 %[x0], v254, %[gx] offset:-4096 sc1\n.endif\n"
    "s_waitcnt vmcnt(%c[w]-1)\n\tv_max3_u32 v255, %[a], %[b], %[c]\n\tv_max_u32 v255, v255, %[d]\n\tv_cmp_eq_u32 vcc, -1, v255\n"
    "s_cbranch_vccz .Lhd%c[id]\n\ts_sleep " STR(RSLP) "\n\ts_sub_u32 s95, s95, 1\n\ts_cmp_lg_u32 s95, 0\n\ts_cbranch_scc1 .Lhp%c[id]\n.Lhd%c[id]:\n"
    "s_branch .Lhb%c[id]\n\t.subsection 0"
    : [x0] "+v"(x0)
    : [m] "s"(m), [xv] "v"(xv), [gx] "s"(gx), [a] "v"(x0[0]), [b] "v"(x0[1]), [c] "v"(x0[2]), [d] "v"(x0[3]), [id] "n"(ID), [w] "n"(W), [off] "n"(OFF)
    : "memory", "scc", "vcc", "s95", "v253", "v254", "v255");
}
template <int ID, int W, int OFF> DI void hole32(u64 m, unsigned xv, const char* gx, u32x4& x0, u32x4& x1) {
  asm volatile("s_cmp_lg_u64 %[m], 0\n\ts_cbranch_scc1 .Lhs%c[id]\n.Lhb%c[id]:\n\t.subsection 1\n.Lhs%c[id]:\n\ts_mov_b32 s95, " STR(SPINMAX) "\n"
    "v_add_u32 v254, %c[off], %[xv]\n.Lhp%c[id]:\n"
    ".if %c[w] > 0\nglobal_load_dwordx4 %[x0], v254, %[gx] offset:-4096 sc1\n.endif\n"
    ".if %c[w] > 1\nglobal_load_dwordx4 %[x1], v254, %[gx] offset:-3072 sc1\n.endif\n"
    "s_waitcnt vmcnt(%c[w]-1)\n\tv_max3_u32 v255, %[a], %[b], %[c]\n\tv_max_u32 v255, v255, %[d]\n\tv_cmp_eq_u32 vcc, -1, v255\n"
    "s_cbranch_vccz .Lhd%c[id]\n\ts_sleep " STR(RSLP) "\n\ts_sub_u32 s95, s95, 1\n\ts_cmp_lg_u32 s95, 0\n\ts_cbranch_scc1 .Lhp%c[id]\n.Lhd%c[id]:\n"
    ".if %c[w] <= 1\nglobal_load_dwordx4 %[x1], v254, %[gx] offset:-3072 sc1\n.endif\n"
    "s_branch .Lhb%c[id]\n\t.subsection 0"
    : [x0] "+v"(x0), [x1] "+v"(x1)
    : [m] "s"(m), [xv] "v"(xv), [gx] "s"(gx), [a] "v"(x0[0]), [b] "v"(x0[1]), [c] "v"(x0[2]), [d] "v"(x0[3]), [id] "n"(ID), [w] "n"(W), [off] "n"(OFF)
    : "memory", "scc", "vcc", "s95", "v253", "v254", "v255");
}
template <int ID, int W, int OFF> DI void hole32(u64 m, unsigned xv, const char* gx, u32x4& x0, u32x4& x1, u32x4& x2) {
  asm volatile("s_cmp_lg_u64 %[m], 0\n\ts_cbranch_scc1 .Lhs%c[id]\n.Lhb%c[id]:\n\t.subsection 1\n.Lhs%c[id]:\n\ts_mov_b32 s95, " STR(SPINMAX) "\n"
    "v_add_u32 v254, %c[off], %[xv]\n.Lhp%c[id]:\n"
    ".if %c[w] > 0\nglobal_load_dwordx4 %[x0], v254, %[gx] offset:-4096 sc1\n.endif\n"
    ".if %c[w] > 1\nglobal_load_dwordx4 %[x1], v254, %[gx] offset:-3072 sc1\n.endif\n"
    ".if %c[w] > 2\nglobal_load_dwordx4 %[x2], v254, %[gx] offset:-2048 sc1\n.endif\n"
    "s_waitcnt vmcnt(%c[w]-1)\n\tv_max3_u32 v255, %[a], %[b], %[c]\n\tv_max_u32 v255, v255, %[d]\n\tv_cmp_eq_u32 vcc, -1, v255\n"
    "s_cbranch_vccz .Lhd%c[id]\n\ts_sleep " STR(RSLP) "\n\ts_sub_u32 s95, s95, 1\n\ts_cmp_lg_u32 s95, 0\n\ts_cbranch_scc1 .Lhp%c[id]\n.Lhd%c[id]:\n"
    ".if %c[w] <= 1\nglobal_load_dwordx4 %[x1], v254, %[gx] offset:-3072 sc1\n.endif\n"
    ".if %c[w] <= 2\nglobal_load_dwordx4 %[x2], v254, %[gx] offset:-2048 sc1\n.endif\n"
    "s_branch .Lhb%c[id]\n\t.subsection 0"
    : [x0] "+v"(x0), [x1] "+v"(x1), [x2] "+v"(x2)
    : [m] "s"(m), [xv] "v"(xv), [gx] "s"(gx), [a] "v"(x0[0]), [b] "v"(x0[1]), [c] "v"(x0[2]), [d] "v"(x0[3]), [id] "n"(ID), [w] "n"(W), [off] "n"(OFF)
    : "memory", "scc", "vcc", "s95", "v253", "v254", "v255");
}
template <int ID, int W, int OFF> DI void hole32(u64 m, unsigned xv, const char* gx, u32x4& x0, u32x4& x1, u32x4& x2, u32x4& x3) {
  asm volatile("s_cmp_lg_u64 %[m], 0\n\ts_cbranch_scc1 .Lhs%c[id]\n.Lhb%c[id]:\n\t.subsection 1\n.Lhs%c[id]:\n\ts_mov_b32 s95, " STR(SPINMAX) "\n"
    "v_add_u32 v254, %c[off], %[xv]\n.Lhp%c[id]:\n"
    ".if %c[w] > 0\nglobal_load_dwordx4 %[x0], v254, %[gx] offset:-4096 sc1\n.endif\n"
    ".if %c[w] > 1\nglobal_load_dwordx4 %[x1], v254, %[gx] offset:-3072 sc1\n.endif\n"
    ".if %c[w] > 2\nglobal_load_dwordx4 %[x2], v254, %[gx] offset:-2048 sc1\n.endif\n"
    ".if %c[w] > 3\nglobal_load_dwordx4 %[x3], v254, %[gx] offset:-1024 sc1\n.endif\n"
    "s_waitcnt vmcnt(%c[w]-1)\n\tv_max3_u32 v255, %[a], %[b], %[c]\n\tv_max_u32 v255, v255, %[d]\n\tv_cmp_eq_u32 vcc, -1, v255\n"
    "s_cbranch_vccz .Lhd%c[id]\n\ts_sleep " STR(RSLP) "\n\ts_sub_u32 s95, s95, 1\n\ts_cmp_lg_u32 s95, 0\n\ts_cbranch_scc1 .Lhp%c[id]\n.Lhd%c[id]:\n"
    ".if %c[w] <= 1\nglobal_load_dwordx4 %[x1], v254, %[gx] offset:-3072 sc1\n.endif\n"
    ".if %c[w] <= 2\nglobal_load_dwordx4 %[x2], v254, %[gx] offset:-2048 sc1\n.endif\n"
    ".if %c[w] <= 3\nglobal_load_dwordx4 %[x3], v254, %[gx] offset:-1024 sc1\n.endif\n"
    "s_branch .Lhb%c[id]\n\t.subsection 0"
    : [x0] "+v"(x0), [x1] "+v"(x1), [x2] "+v"(x2), [x3] "+v"(x3)
    : [m] "s"(m), [xv] "v"(xv), [gx] "s"(gx), [a] "v"(x0[0]), [b] "v"(x0[1]), [c] "v"(x0[2]), [d] "v"(x0[3]), [id] "n"(ID), [w] "n"(W), [off] "n"(OFF)
    : "memory", "scc", "vcc", "s95", "v253", "v254", "v255");
}
template <int ID, int W, int OFF> DI void hole32(u64 m, unsigned xv, const char* gx, u32x4& x0, u32x4& x1, u32x4& x2, u32x4& x3, u32x4& x4) {
  asm volatile("s_cmp_lg_u64 %[m], 0\n\ts_cbranch_scc1 .Lhs%c[id]\n.Lhb%c[id]:\n\t.subsection 1\n.Lhs%c[id]:\n\ts_mov_b32 s95, " STR(SPINMAX) "\n"
    "v_add_u32 v254, %c[off], %[xv]\n.Lhp%c[id]:\n"
    ".if %c[w] > 0\nglobal_load_dwordx4 %[x0], v254, %[gx] offset:-4096 sc1\n.endif\n"
    ".if %c[w] > 1\nglobal_load_dwordx4 %[x1], v254, %[gx] offset:-3072 sc1\n.endif\n"
    ".if %c[w] > 2\nglobal_load_dwordx4 %[x2], v254, %[gx] offset:-2048 sc1\n.endif\n"
    ".if %c[w] > 3\nglobal_load_dwordx4 %[x3], v254, %[gx] offset:-1024 sc1\n.endif\n"
    ".if %c[w] > 4\nglobal_load_dwordx4 %[x4], v254, %[gx] offset:0 sc1\n.endif\n"
    "s_waitcnt vmcnt(%c[w]-1)\n\tv_max3_u32 v255, %[a], %[b], %[c]\n\tv_max_u32 v255, v255, %[d]\n\tv_cmp_eq_u32 vcc, -1, v255\n"
    "s_cbranch_vccz .Lhd%c[id]\n\ts_sleep " STR(RSLP) "\n\ts_sub_u32 s95, s95, 1\n\ts_cmp_lg_u32 s95, 0\n\ts_cbranch_scc1 .Lhp%c[id]\n.Lhd%c[id]:\n"
    ".if %c[w] <= 1\nglobal_load_dwordx4 %[x1], v254, %[gx] offset:-3072 sc1\n.endif\n"
    ".if %c[w] <= 2\nglobal_load_dwordx4 %[x2], v254, %[gx] offset:-2048 sc1\n.endif\n"
    ".if %c[w] <= 3\nglobal_load_dwordx4 %[x3], v254, %[gx] offset:-1024 sc1\n.endif\n"
    ".if %c[w] <= 4\nglobal_load_dwordx4 %[x4], v254, %[gx] offset:0 sc1\n.endif\n"
    "s_branch .Lhb%c[id]\n\t.subsection 0"
    : [x0] "+v"(x0), [x1] "+v"(x1), [x2] "+v"(x2), [x3] "+v"(x3), [x4] "+v"(x4)
    : [m] "s"(m), [xv] "v"(xv), [gx] "s"(gx), [a] "v"(x0[0]), [b] "v"(x0[1]), [c] "v"(x0[2]), [d] "v"(x0[3]), [id] "n"(ID), [w] "n"(W), [off] "n"(OFF)
    : "memory", "scc", "vcc", "s95", "v253", "v254", "v255");
}
template <int ID, int W, int OFF> DI void hole32(u64 m, unsigned xv, const char* gx, u32x4& x0, u32x4& x1, u32x4& x2, u32x4& x3, u32x4& x4, u32x4& x5) {
  asm volatile("s_cmp_lg_u64 %[m], 0\n\ts_cbranch_scc1 .Lhs%c[id]\n.Lhb%c[id]:\n\t.subsection 1\n.Lhs%c[id]:\n\ts_mov_b32 s95, " STR(SPINMAX) "\n"
    "v_add_u32 v254, %c[off], %[xv]\n.Lhp%c[id]:\n"
    ".if %c[w] > 0\nglobal_load_dwordx4 %[x0], v254, %[gx] offset:-4096 sc1\n.endif\n"
    ".if %c[w] > 1\nglobal_load_dwordx4 %[x1], v254, %[gx] offset:-3072 sc1\n.endif\n"
    ".if %c[w] > 2\nglobal_load_dwordx4 %[x2], v254, %[gx] offset:-2048 sc1\n.endif\n"
    ".if %c[w] > 3\nglobal_load_dwordx4 %[x3], v254, %[gx] offset:-1024 sc1\n.endif\n"
    ".if %c[w] > 4\nglobal_load_dwordx4 %[x4], v254, %[gx] offset:0 sc1\n.endif\n"
    ".if %c[w] > 5\nglobal_load_dwordx4 %[x5], v254, %[gx] offset:1024 sc1\n.endif\n"
    "s_waitcnt vmcnt(%c[w]-1)\n\tv_max3_u32 v255, %[a], %[b], %[c]\n\tv_max_u32 v255, v255, %[d]\n\tv_cmp_eq_u32 vcc, -1, v255\n"
    "s_cbranch_vccz .Lhd%c[id]\n\ts_sleep " STR(RSLP) "\n\ts_sub_u32 s95, s95, 1\n\ts_cmp_lg_u32 s95, 0\n\ts_cbranch_scc1 .Lhp%c[id]\n.Lhd%c[id]:\n"
    ".if %c[w] <= 1\nglobal_load_dwordx4 %[x1], v254, %[gx] offset:-3072 sc1\n.endif\n"
    ".if %c[w] <= 2\nglobal_load_dwordx4 %[x2], v254, %[gx] offset:-2048 sc1\n.endif\n"
    ".if %c[w] <= 3\nglobal_load_dwordx4 %[x3], v254, %[gx] offset:-1024 sc1\n.endif\n"
    ".if %c[w] <= 4\nglobal_load_dwordx4 %[x4], v254, %[gx] offset:0 sc1\n.endif\n"
    ".if %c[w] <= 5\nglobal_load_dwordx4 %[x5], v254, %[gx] offset:1024 sc1\n.endif\n"
    "s_branch .Lhb%c[id]\n\t.subsection 0"
    : [x0] "+v"(x0), [x1] "+v"(x1), [x2] "+v"(x2), [x3] "+v"(x3), [x4] "+v"(x4), [x5] "+v"(x5)
    : [m] "s"(m), [xv] "v"(xv), [gx] "s"(gx), [a] "v"(x0[0]), [b] "v"(x0[1]), [c] "v"(x0[2]), [d] "v"(x0[3]), [id] "n"(ID), [w] "n"(W), [off] "n"(OFF)
    : "memory", "scc", "vcc", "s95", "v253", "v254", "v255");
}
template <int ID, int W, int OFF> DI void hole32(u64 m, unsigned xv, const char* gx, u32x4& x0, u32x4& x1, u32x4& x2, u32x4& x3, u32x4& x4, u32x4& x5, u32x4& x6) {
  asm volatile("s_cmp_lg_u64 %[m], 0\n\ts_cbranch_scc1 .Lhs%c[id]\n.Lhb%c[id]:\n\t.subsection 1\n.Lhs%c[id]:\n\ts_mov_b32 s95, " STR(SPINMAX) "\n"
    "v_add_u32 v254, %c[off], %[xv]\n.Lhp%c[id]:\n"
    ".if %c[w] > 0\nglobal_load_dwordx4 %[x0], v254, %[gx] offset:-4096 sc1\n.endif\n"
    ".if %c[w] > 1\nglobal_load_dwordx4 %[x1], v254, %[gx] offset:-3072 sc1\n.endif\n"
    ".if %c[w] > 2\nglobal_load_dwordx4 %[x2], v254, %[gx] offset:-2048 sc1\n.endif\n"
    ".if %c[w] > 3\nglobal_load_dwordx4 %[x3], v254, %[gx] offset:-1024 sc1\n.endif\n"
    ".if %c[w] > 4\nglobal_load_dwordx4 %[x4], v254, %[gx] offset:0 sc1\n.endif\n"
    ".if %c[w] > 5\nglobal_load_dwordx4 %[x5], v254, %[gx] offset:1024 sc1\n.endif\n"
    ".if %c[w] > 6\nglobal_load_dwordx4 %[x6], v254, %[gx] offset:2048 sc1\n.endif\n"
    "s_waitcnt vmcnt(%c[w]-1)\n\tv_max3_u32 v255, %[a], %[b], %[c]\n\tv_max_u32 v255, v255, %[d]\n\tv_cmp_eq_u32 vcc, -1, v255\n"
    "s_cbranch_vccz .Lhd%c[id]\n\ts_sleep " STR(RSLP) "\n\ts_sub_u32 s95, s95, 1\n\ts_cmp_lg_u32 s95, 0\n\ts_cbranch_scc1 .Lhp%c[id]\n.Lhd%c[id]:\n"
    ".if %c[w] <= 1\nglobal_load_dwordx4 %[x1], v254, %[gx] offset:-3072 sc1\n.endif\n"
    ".if %c[w] <= 2\nglobal_load_dwordx4 %[x2], v254, %[gx] offset:-2048 sc1\n.endif\n"
    ".if %c[w] <= 3\nglobal_load_dwordx4 %[x3], v254, %[gx] offset:-1024 sc1\n.endif\n"
    ".if %c[w] <= 4\nglobal_load_dwordx4 %[x4], v254, %[gx] offset:0 sc1\n.endif\n"
    ".if %c[w] <= 5\nglobal_load_dwordx4 %[x5], v254, %[gx] offset:1024 sc1\n.endif\n"
    ".if %c[w] <= 6\nglobal_load_dwordx4 %[x6], v254, %[gx] offset:2048 sc1\n.endif\n"
    "s_branch .Lhb%c[id]\n\t.subsection 0"
    : [x0] "+v"(x0), [x1] "+v"(x1), [x2] "+v"(x2), [x3] "+v"(x3), [x4] "+v"(x4), [x5] "+v"(x5), [x6] "+v"(x6)
    : [m] "s"(m), [xv] "v"(xv), [gx] "s"(gx), [a] "v"(x0[0]), [b] "v"(x0[1]), [c] "v"(x0[2]), [d] "v"(x0[3]), [id] "n"(ID), [w] "n"(W), [off] "n"(OFF)
    : "memory", "scc", "vcc", "s95", "v253", "v254", "v255");
}
template <int ID, int W, int OFF> DI void hole32(u64 m, unsigned xv, const char* gx, u32x4& x0, u32x4& x1, u32x4& x2, u32x4& x3, u32x4& x4, u32x4& x5, u32x4& x6, u32x4& x7) {
  asm volatile("s_cmp_lg_u64 %[m], 0\n\ts_cbranch_scc1 .Lhs%c[id]\n.Lhb%c[id]:\n\t.subsection 1\n.Lhs%c[id]:\n\ts_mov_b32 s95, " STR(SPINMAX) "\n"
    "v_add_u32 v254, %c[off], %[xv]\n.Lhp%c[id]:\n"
    ".if %c[w] > 0\nglobal_load_dwordx4 %[x0], v254, %[gx] offset:-4096 sc1\n.endif\n"
    ".if %c[w] > 1\nglobal_load_dwordx4 %[x1], v254, %[gx] offset:-3072 sc1\n.endif\n"
    ".if %c[w] > 2\nglobal_load_dwordx4 %[x2], v254, %[gx] offset:-2048 sc1\n.endif\n"
    ".if %c[w] > 3\nglobal_load_dwordx4 %[x3], v254, %[gx] offset:-1024 sc1\n.endif\n"
    ".if %c[w] > 4\nglobal_load_dwordx4 %[x4], v254, %[gx] offset:0 sc1\n.endif\n"
    ".if %c[w] > 5\nglobal_load_dwordx4 %[x5], v254, %[gx] offset:1024 sc1\n.endif\n"
    ".if %c[w] > 6\nglobal_load_dwordx4 %[x6], v254, %[gx] offset:2048 sc1\n.endif\n"
    ".if %c[w] > 7\nglobal_load_dwordx4 %[x7], v254, %[gx] offset:3072 sc1\n.endif\n"
    "s_waitcnt vmcnt(%c[w]-1)\n\tv_max3_u32 v255, %[a], %[b], %[c]\n\tv_max_u32 v255, v255, %[d]\n\tv_cmp_eq_u32 vcc, -1, v255\n"
    "s_cbranch_vccz .Lhd%c[id]\n\ts_sleep " STR(RSLP) "\n\ts_sub_u32 s95, s95, 1\n\ts_cmp_lg_u32 s95, 0\n\ts_cbranch_scc1 .Lhp%c[id]\n.Lhd%c[id]:\n"
    ".if %c[w] <= 1\nglobal_load_dwordx4 %[x1], v254, %[gx] offset:-3072 sc1\n.endif\n"
    ".if %c[w] <= 2\nglobal_load_dwordx4 %[x2], v254, %[gx] offset:-2048 sc1\n.endif\n"
    ".if %c[w] <= 3\nglobal_load_dwordx4 %[x3], v254, %[gx] offset:-1024 sc1\n.endif\n"
    ".if %c[w] <= 4\nglobal_load_dwordx4 %[x4], v254, %[gx] offset:0 sc1\n.endif\n"
    ".if %c[w] <= 5\nglobal_load_dwordx4 %[x5], v254, %[gx] offset:1024 sc1\n.endif\n"
    ".if %c[w] <= 6\nglobal_load_dwordx4 %[x6], v254, %[gx] offset:2048 sc1\n.endif\n"
    ".if %c[w] <= 7\nglobal_load_dwordx4 %[x7], v254, %[gx] offset:3072 sc1\n.endif\n"
    "s_branch .Lhb%c[id]\n\t.subsection 0"
    : [x0] "+v"(x0), [x1] "+v"(x1), [x2] "+v"(x2), [x3] "+v"(x3), [x4] "+v"(x4), [x5] "+v"(x5), [x6] "+v"(x6), [x7] "+v"(x7)
    : [m] "s"(m), [xv] "v"(xv), [gx] "s"(gx), [a] "v"(x0[0]), [b] "v"(x0[1]), [c] "v"(x0[2]), [d] "v"(x0[3]), [id] "n"(ID), [w] "n"(W), [off] "n"(OFF)
    : "memory", "scc", "vcc", "s95", "v253", "v254", "v255");
}
template <int ID, int W, int OFF> DI void hole32(u64 m, unsigned xv, const char* gx, u32x4& x0, u32x4& x1, u32x4& x2, u32x4& x3, u32x4& x4, u32x4& x5, u32x4& x6, u32x4& x7, u32x4& x8) {
  asm volatile("s_cmp_lg_u64 %[m], 0\n\ts_cbranch_scc1 .Lhs%c[id]\n.Lhb%c[id]:\n\t.subsection 1\n.Lhs%c[id]:\n\ts_mov_b32 s95, " STR(SPINMAX) "\n"
    "v_add_u32 v254, %c[off], %[xv]\n\tv_add_u32 v253, %c[off]+8192, %[xv]\n.Lhp%c[id]:\n"
    ".if %c[w] > 0\nglobal_load_dwordx4 %[x0], v254, %[gx] offset:-4096 sc1\n.endif\n"
    ".if %c[w] > 1\nglobal_load_dwordx4 %[x1], v254, %[gx] offset:-3072 sc1\n.endif\n"
    ".if %c[w] > 2\nglobal_load_dwordx4 %[x2], v254, %[gx] offset:-2048 sc1\n.endif\n"
    ".if %c[w] > 3\nglobal_load_dwordx4 %[x3], v254, %[gx] offset:-1024 sc1\n.endif\n"
    ".if %c[w] > 4\nglobal_load_dwordx4 %[x4], v254, %[gx] offset:0 sc1\n.endif\n"
    ".if %c[w] > 5\nglobal_load_dwordx4 %[x5], v254, %[gx] offset:1024 sc1\n.endif\n"
    ".if %c[w] > 6\nglobal_load_dwordx4 %[x6], v254, %[gx] offset:2048 sc1\n.endif\n"
    ".if %c[w] > 7\nglobal_load_dwordx4 %[x7], v254, %[gx] offset:3072 sc1\n.endif\n"
    ".if %c[w] > 8\nglobal_load_dwordx4 %[x8], v253, %[gx] offset:-4096 sc1\n.endif\n"
    "s_waitcnt vmcnt(%c[w]-1)\n\tv_max3_u32 v255, %[a], %[b], %[c]\n\tv_max_u32 v255, v255, %[d]\n\tv_cmp_eq_u32 vcc, -1, v255\n"
    "s_cbranch_vccz .Lhd%c[id]\n\ts_sleep " STR(RSLP) "\n\ts_sub_u32 s95, s95, 1\n\ts_cmp_lg_u32 s95, 0\n\ts_cbranch_scc1 .Lhp%c[id]\n.Lhd%c[id]:\n"
    ".if %c[w] <= 1\nglobal_load_dwordx4 %[x1], v254, %[gx] offset:-3072 sc1\n.endif\n"
    ".if %c[w] <= 2\nglobal_load_dwordx4 %[x2], v254, %[gx] offset:-2048 sc1\n.endif\n"
    ".if %c[w] <= 3\nglobal_load_dwordx4 %[x3], v254, %[gx] offset:-1024 sc1\n.endif\n"
    ".if %c[w] <= 4\nglobal_load_dwordx4 %[x4], v254, %[gx] offset:0 sc1\n.endif\n"
    ".if %c[w] <= 5\nglobal_load_dwordx4 %[x5], v254, %[gx] offset:1024 sc1\n.endif\n"
    ".if %c[w] <= 6\nglobal_load_dwordx4 %[x6], v254, %[gx] offset:2048 sc1\n.endif\n"
    ".if %c[w] <= 7\nglobal_load_dwordx4 %[x7], v254, %[gx] offset:3072 sc1\n.endif\n"
    ".if %c[w] <= 8\nglobal_load_dwordx4 %[x8], v253, %[gx] offset:-4096 sc1\n.endif\n"
    "s_branch .Lhb%c[id]\n\t.subsection 0"
    : [x0] "+v"(x0), [x1] "+v"(x1), [x2] "+v"(x2), [x3] "+v"(x3), [x4] "+v"(x4), [x5] "+v"(x5), [x6] "+v"(x6), [x7] "+v"(x7), [x8] "+v"(x8)
    : [m] "s"(m), [xv] "v"(xv), [gx] "s"(gx), [a] "v"(x0[0]), [b] "v"(x0[1]), [c] "v"(x0[2]), [d] "v"(x0[3]), [id] "n"(ID), [w] "n"(W), [off] "n"(OFF)
    : "memory", "scc", "vcc", "s95", "v253", "v254", "v255");
}
template <int ID, int W, int OFF> DI void hole32(u64 m, unsigned xv, const char* gx, u32x4& x0, u32x4& x1, u32x4& x2, u32x4& x3, u32x4& x4, u32x4& x5, u32x4& x6, u32x4& x7, u32x4& x8, u32x4& x9) {
  asm volatile("s_cmp_lg_u64 %[m], 0\n\ts_cbranch_scc1 .Lhs%c[id]\n.Lhb%c[id]:\n\t.subsection 1\n.Lhs%c[id]:\n\ts_mov_b32 s95, " STR(SPINMAX) "\n"
    "v_add_u32 v254, %c[off], %[xv]\n\tv_add_u32 v253, %c[off]+8192, %[xv]\n.Lhp%c[id]:\n"
    ".if %c[w] > 0\nglobal_load_dwordx4 %[x0], v254, %[gx] offset:-4096 sc1\n.endif\n"
    ".if %c[w] > 1\nglobal_load_dwordx4 %[x1], v254, %[gx] offset:-3072 sc1\n.endif\n"
    ".if %c[w] > 2\nglobal_load_dwordx4 %[x2], v254, %[gx] offset:-2048 sc1\n.endif\n"
    ".if %c[w] > 3\nglobal_load_dwordx4 %[x3], v254, %[gx] offset:-1024 sc1\n.endif\n"
    ".if %c[w] > 4\nglobal_load_dwordx4 %[x4], v254, %[gx] offset:0 sc1\n.endif\n"
    ".if %c[w] > 5\nglobal_load_dwordx4 %[x5], v254, %[gx] offset:1024 sc1\n.endif\n"
    ".if %c[w] > 6\nglobal_load_dwordx4 %[x6], v254, %[gx] offset:2048 sc1\n.endif\n"
    ".if %c[w] > 7\nglobal_load_dwordx4 %[x7], v254, %[gx] offset:3072 sc1\n.endif\n"
    ".if %c[w] > 8\nglobal_load_dwordx4 %[x8], v253, %[gx] offset:-4096 sc1\n.endif\n"
    ".if %c[w] > 9\nglobal_load_dwordx4 %[x9], v253, %[gx] offset:-3072 sc1\n.endif\n"
    "s_waitcnt vmcnt(%c[w]-1)\n\tv_max3_u32 v255, %[a], %[b], %[c]\n\tv_max_u32 v255, v255, %[d]\n\tv_cmp_eq_u32 vcc, -1, v255\n"
    "s_cbranch_vccz .Lhd%c[id]\n\ts_sleep " STR(RSLP) "\n\ts_sub_u32 s95, s95, 1\n\ts_cmp_lg_u32 s95, 0\n\ts_cbranch_scc1 .Lhp%c[id]\n.Lhd%c[id]:\n"
    ".if %c[w] <= 1\nglobal_load_dwordx4 %[x1], v254, %[gx] offset:-3072 sc1\n.endif\n"
    ".if %c[w] <= 2\nglobal_load_dwordx4 %[x2], v254, %[gx] offset:-2048 sc1\n.endif\n"
    ".if %c[w] <= 3\nglobal_load_dwordx4 %[x3], v254, %[gx] offset:-1024 sc1\n.endif\n"
    ".if %c[w] <= 4\nglobal_load_dwordx4 %[x4], v254, %[gx] offset:0 sc1\n.endif\n"
    ".if %c[w] <= 5\nglobal_load_dwordx4 %[x5], v254, %[gx] offset:1024 sc1\n.endif\n"
    ".if %c[w] <= 6\nglobal_load_dwordx4 %[x6], v254, %[gx] offset:2048 sc1\n.endif\n"
    ".if %c[w] <= 7\nglobal_load_dwordx4 %[x7], v254, %[gx] offset:3072 sc1\n.endif\n"
    ".if %c[w] <= 8\nglobal_load_dwordx4 %[x8], v253, %[gx] offset:-4096 sc1\n.endif\n"
    ".if %c[w] <= 9\nglobal_load_dwordx4 %[x9], v253, %[gx] offset:-3072 sc1\n.endif\n"
    "s_branch .Lhb%c[id]\n\t.subsection 0"
    : [x0] "+v"(x0), [x1] "+v"(x1), [x2] "+v"(x2), [x3] "+v"(x3), [x4] "+v"(x4), [x5] "+v"(x5), [x6] "+v"(x6), [x7] "+v"(x7), [x8] "+v"(x8), [x9] "+v"(x9)
    : [m] "s"(m), [xv] "v"(xv), [gx] "s"(gx), [a] "v"(x0[0]), [b] "v"(x0[1]), [c] "v"(x0[2]), [d] "v"(x0[3]), [id] "n"(ID), [w] "n"(W), [off] "n"(OFF)
    : "memory", "scc", "vcc", "s95", "v253", "v254", "v255");
}
template <int ID, int W, int OFF> DI void hole32(u64 m, unsigned xv, const char* gx, u32x4& x0, u32x4& x1, u32x4& x2, u32x4& x3, u32x4& x4, u32x4& x5, u32x4& x6, u32x4& x7, u32x4& x8, u32x4& x9, u32x4& x10) {
  asm volatile("s_cmp_lg_u64 %[m], 0\n\ts_cbranch_scc1 .Lhs%c[id]\n.Lhb%c[id]:\n\t.subsection 1\n.Lhs%c[id]:\n\ts_mov_b32 s95, " STR(SPINMAX) "\n"
    "v_add_u32 v254, %c[off], %[xv]\n\tv_add_u32 v253, %c[off]+8192, %[xv]\n.Lhp%c[id]:\n"
    ".if %c[w] > 0\nglobal_load_dwordx4 %[x0], v254, %[gx] offset:-4096 sc1\n.endif\n"
    ".if %c[w] > 1\nglobal_load_dwordx4 %[x1], v254, %[gx] offset:-3072 sc1\n.endif\n"
    ".if %c[w] > 2\nglobal_load_dwordx4 %[x2], v254, %[gx] offset:-2048 sc1\n.endif\n"
    ".if %c[w] > 3\nglobal_load_dwordx4 %[x3], v254, %[gx] offset:-1024 sc1\n.endif\n"
    ".if %c[w] > 4\nglobal_load_dwordx4 %[x4], v254, %[gx] offset:0 sc1\n.endif\n"
    ".if %c[w] > 5\nglobal_load_dwordx4 %[x5], v254, %[gx] offset:1024 sc1\n.endif\n"
    ".if %c[w] > 6\nglobal_load_dwordx4 %[x6], v254, %[gx] offset:2048 sc1\n.endif\n"
    ".if %c[w] > 7\nglobal_load_dwordx4 %[x7], v254, %[gx] offset:3072 sc1\n.endif\n"
    ".if %c[w] > 8\nglobal_load_dwordx4 %[x8], v253, %[gx] offset:-4096 sc1\n.endif\n"
    ".if %c[w] > 9\nglobal_load_dwordx4 %[x9], v253, %[gx] offset:-3072 sc1\n.endif\n"
    ".if %c[w] > 10\nglobal_load_dwordx4 %[x10], v253, %[gx] offset:-2048 sc1\n.endif\n"
    "s_waitcnt vmcnt(%c[w]-1)\n\tv_max3_u32 v255, %[a], %[b], %[c]\n\tv_max_u32 v255, v255, %[d]\n\tv_cmp_eq_u32 vcc, -1, v255\n"
    "s_cbranch_vccz .Lhd%c[id]\n\ts_sleep " STR(RSLP) "\n\ts_sub_u32 s95, s95, 1\n\ts_cmp_lg_u32 s95, 0\n\ts_cbranch_scc1 .Lhp%c[id]\n.Lhd%c[id]:\n"
    ".if %c[w] <= 1\nglobal_load_dwordx4 %[x1], v254, %[gx] offset:-3072 sc1\n.endif\n"
    ".if %c[w] <= 2\nglobal_load_dwordx4 %[x2], v254, %[gx] offset:-2048 sc1\n.endif\n"
    ".if %c[w] <= 3\nglobal_load_dwordx4 %[x3], v254, %[gx] offset:-1024 sc1\n.endif\n"
    ".if %c[w] <= 4\nglobal_load_dwordx4 %[x4], v254, %[gx] offset:0 sc1\n.endif\n"
    ".if %c[w] <= 5\nglobal_load_dwordx4 %[x5], v254, %[gx] offset:1024 sc1\n.endif\n"
    ".if %c[w] <= 6\nglobal_load_dwordx4 %[x6], v254, %[gx] offset:2048 sc1\n.endif\n"
    ".if %c[w] <= 7\nglobal_load_dwordx4 %[x7], v254, %[gx] offset:3072 sc1\n.endif\n"
    ".if %c[w] <= 8\nglobal_load_dwordx4 %[x8], v253, %[gx] offset:-4096 sc1\n.endif\n"
    ".if %c[w] <= 9\nglobal_load_dwordx4 %[x9], v253, %[gx] offset:-3072 sc1\n.endif\n"
    ".if %c[w] <= 10\nglobal_load_dwordx4 %[x10], v253, %[gx] offset:-2048 sc1\n.endif\n"
    "s_branch .Lhb%c[id]\n\t.subsection 0"
    : [x0] "+v"(x0), [x1] "+v"(x1), [x2] "+v"(x2), [x3] "+v"(x3), [x4] "+v"(x4), [x5] "+v"(x5), [x6] "+v"(x6), [x7] "+v"(x7), [x8] "+v"(x8), [x9] "+v"(x9), [x10] "+v"(x10)
    : [m] "s"(m), [xv] "v"(xv), [gx] "s"(gx), [a] "v"(x0[0]), [b] "v"(x0[1]), [c] "v"(x0[2]), [d] "v"(x0[3]), [id] "n"(ID), [w] "n"(W), [off] "n"(OFF)
    : "memory", "scc", "vcc", "s95", "v253", "v254", "v255");
}
template <int ID, int W, int OFF> DI void hole32(u64 m, unsigned xv, const char* gx, u32x4& x0, u32x4& x1, u32x4& x2, u32x4& x3, u32x4& x4, u32x4& x5, u32x4& x6, u32x4& x7, u32x4& x8, u32x4& x9, u32x4& x10, u32x4& x11) {
  asm volatile("s_cmp_lg_u64 %[m], 0\n\ts_cbranch_scc1 .Lhs%c[id]\n.Lhb%c[id]:\n\t.subsection 1\n.Lhs%c[id]:\n\ts_mov_b32 s95, " STR(SPINMAX) "\n"
    "v_add_u32 v254, %c[off], %[xv]\n\tv_add_u32 v253, %c[off]+8192, %[xv]\n.Lhp%c[id]:\n"
    ".if %c[w] > 0\nglobal_load_dwordx4 %[x0], v254, %[gx] offset:-4096 sc1\n.endif\n"
    ".if %c[w] > 1\nglobal_load_dwordx4 %[x1], v254, %[gx] offset:-3072 sc1\n.endif\n"
    ".if %c[w] > 2\nglobal_load_dwordx4 %[x2], v254, %[gx] offset:-2048 sc1\n.endif\n"
    ".if %c[w] > 3\nglobal_load_dwordx4 %[x3], v254, %[gx] offset:-1024 sc1\n.endif\n"
    ".if %c[w] > 4\nglobal_load_dwordx4 %[x4], v254, %[gx] offset:0 sc1\n.endif\n"
    ".if %c[w] > 5\nglobal_load_dwordx4 %[x5], v254, %[gx] offset:1024 sc1\n.endif\n"
    ".if %c[w] > 6\nglobal_load_dwordx4 %[x6], v254, %[gx] offset:2048 sc1\n.endif\n"
    ".if %c[w] > 7\nglobal_load_dwordx4 %[x7], v254, %[gx] offset:3072 sc1\n.endif\n"
    ".if %c[w] > 8\nglobal_load_dwordx4 %[x8], v253, %[gx] offset:-4096 sc1\n.endif\n"
    ".if %c[w] > 9\nglobal_load_dwordx4 %[x9], v253, %[gx] offset:-3072 sc1\n.endif\n"
    ".if %c[w] > 10\nglobal_load_dwordx4 %[x10], v253, %[gx] offset:-2048 sc1\n.endif\n"
    ".if %c[w] > 11\nglobal_load_dwordx4 %[x11], v253, %[gx] offset:-1024 sc1\n.endif\n"
    "s_waitcnt vmcnt(%c[w]-1)\n\tv_max3_u32 v255, %[a], %[b], %[c]\n\tv_max_u32 v255, v255, %[d]\n\tv_cmp_eq_u32 vcc, -1, v255\n"
    "s_cbranch_vccz .Lhd%c[id]\n\ts_sleep " STR(RSLP) "\n\ts_sub_u32 s95, s95, 1\n\ts_cmp_lg_u32 s95, 0\n\ts_cbranch_scc1 .Lhp%c[id]\n.Lhd%c[id]:\n"
    ".if %c[w] <= 1\nglobal_load_dwordx4 %[x1], v254, %[gx] offset:-3072 sc1\n.endif\n"
    ".if %c[w] <= 2\nglobal_load_dwordx4 %[x2], v254, %[gx] offset:-2048 sc1\n.endif\n"
    ".if %c[w] <= 3\nglobal_load_dwordx4 %[x3], v254, %[gx] offset:-1024 sc1\n.endif\n"
    ".if %c[w] <= 4\nglobal_load_dwordx4 %[x4], v254, %[gx] offset:0 sc1\n.endif\n"
    ".if %c[w] <= 5\nglobal_load_dwordx4 %[x5], v254, %[gx] offset:1024 sc1\n.endif\n"
    ".if %c[w] <= 6\nglobal_load_dwordx4 %[x6], v254, %[gx] offset:2048 sc1\n.endif\n"
    ".if %c[w] <= 7\nglobal_load_dwordx4 %[x7], v254, %[gx] offset:3072 sc1\n.endif\n"
    ".if %c[w] <= 8\nglobal_load_dwordx4 %[x8], v253, %[gx] offset:-4096 sc1\n.endif\n"
    ".if %c[w] <= 9\nglobal_load_dwordx4 %[x9], v253, %[gx] offset:-3072 sc1\n.endif\n"
    ".if %c[w] <= 10\nglobal_load_dwordx4 %[x10], v253, %[gx] offset:-2048 sc1\n.endif\n"
    ".if %c[w] <= 11\nglobal_load_dwordx4 %[x11], v253, %[gx] offset:-1024 sc1\n.endif\n"
    "s_branch .Lhb%c[id]\n\t.subsection 0"
    : [x0] "+v"(x0), [x1] "+v"(x1), [x2] "+v"(x2), [x3] "+v"(x3), [x4] "+v"(x4), [x5] "+v"(x5), [x6] "+v"(x6), [x7] "+v"(x7), [x8] "+v"(x8), [x9] "+v"(x9), [x10] "+v"(x10), [x11] "+v"(x11)
    : [m] "s"(m), [xv] "v"(xv), [gx] "s"(gx), [a] "v"(x0[0]), [b] "v"(x0[1]), [c] "v"(x0[2]), [d] "v"(x0[3]), [id] "n"(ID), [w] "n"(W), [off] "n"(OFF)
    : "memory", "scc", "vcc", "s95", "v253", "v254", "v255");
}
template <int ID, int W, int OFF> DI void hole32(u64 m, unsigned xv, const char* gx, u32x4& x0, u32x4& x1, u32x4& x2, u32x4& x3, u32x4& x4, u32x4& x5, u32x4& x6, u32x4& x7, u32x4& x8, u32x4& x9, u32x4& x10, u32x4& x11, u32x4& x12) {
  asm volatile("s_cmp_lg_u64 %[m], 0\n\ts_cbranch_scc1 .Lhs%c[id]\n.Lhb%c[id]:\n\t.subsection 1\n.Lhs%c[id]:\n\ts_mov_b32 s95, " STR(SPINMAX) "\n"
    "v_add_u32 v254, %c[off], %[xv]\n\tv_add_u32 v253, %c[off]+8192, %[xv]\n.Lhp%c[id]:\n"
    ".if %c[w] > 0\nglobal_load_dwordx4 %[x0], v254, %[gx] offset:-4096 sc1\n.endif\n"
    ".if %c[w] > 1\nglobal_load_dwordx4 %[x1], v254, %[gx] offset:-3072 sc1\n.endif\n"
    ".if %c[w] > 2\nglobal_load_dwordx4 %[x2], v254, %[gx] offset:-2048 sc1\n.endif\n"
    ".if %c[w] > 3\nglobal_load_dwordx4 %[x3], v254, %[gx] offset:-1024 sc1\n.endif\n"
    ".if %c[w] > 4\nglobal_load_dwordx4 %[x4], v254, %[gx] offset:0 sc1\n.endif\n"
    ".if %c[w] > 5\nglobal_load_dwordx4 %[x5], v254, %[gx] offset:1024 sc1\n.endif\n"
    ".if %c[w] > 6\nglobal_load_dwordx4 %[x6], v254, %[gx] offset:2048 sc1\n.endif\n"
    ".if %c[w] > 7\nglobal_load_dwordx4 %[x7], v254, %[gx] offset:3072 sc1\n.endif\n"
    ".if %c[w] > 8\nglobal_load_dwordx4 %[x8], v253, %[gx] offset:-4096 sc1\n.endif\n"
    ".if %c[w] > 9\nglobal_load_dwordx4 %[x9], v253, %[gx] offset:-3072 sc1\n.endif\n"
    ".if %c[w] > 10\nglobal_load_dwordx4 %[x10], v253, %[gx] offset:-2048 sc1\n.endif\n"
    ".if %c[w] > 11\nglobal_load_dwordx4 %[x11], v253, %[gx] offset:-1024 sc1\n.endif\n"
    ".if %c[w] > 12\nglobal_load_dwordx4 %[x12], v253, %[gx] offset:0 sc1\n.endif\n"
    "s_waitcnt vmcnt(%c[w]-1)\n\tv_max3_u32 v255, %[a], %[b], %[c]\n\tv_max_u32 v255, v255, %[d]\n\tv_cmp_eq_u32 vcc, -1, v255\n"
    "s_cbranch_vccz .Lhd%c[id]\n\ts_sleep " STR(RSLP) "\n\ts_sub_u32 s95, s95, 1\n\ts_cmp_lg_u32 s95, 0\n\ts_cbranch_scc1 .Lhp%c[id]\n.Lhd%c[id]:\n"
    ".if %c[w] <= 1\nglobal_load_dwordx4 %[x1], v254, %[gx] offset:-3072 sc1\n.endif\n"
    ".if %c[w] <= 2\nglobal_load_dwordx4 %[x2], v254, %[gx] offset:-2048 sc1\n.endif\n"
    ".if %c[w] <= 3\nglobal_load_dwordx4 %[x3], v254, %[gx] offset:-1024 sc1\n.endif\n"
    ".if %c[w] <= 4\nglobal_load_dwordx4 %[x4], v254, %[gx] offset:0 sc1\n.endif\n"
    ".if %c[w] <= 5\nglobal_load_dwordx4 %[x5], v254, %[gx] offset:1024 sc1\n.endif\n"
    ".if %c[w] <= 6\nglobal_load_dwordx4 %[x6], v254, %[gx] offset:2048 sc1\n.endif\n"
    ".if %c[w] <= 7\nglobal_load_dwordx4 %[x7], v254, %[gx] offset:3072 sc1\n.endif\n"
    ".if %c[w] <= 8\nglobal_load_dwordx4 %[x8], v253, %[gx] offset:-4096 sc1\n.endif\n"
    ".if %c[w] <= 9\nglobal_load_dwordx4 %[x9], v253, %[gx] offset:-3072 sc1\n.endif\n"
    ".if %c[w] <= 10\nglobal_load_dwordx4 %[x10], v253, %[gx] offset:-2048 sc1\n.endif\n"
    ".if %c[w] <= 11\nglobal_load_dwordx4 %[x11], v253, %[gx] offset:-1024 sc1\n.endif\n"
    ".if %c[w] <= 12\nglobal_load_dwordx4 %[x12], v253, %[gx] offset:0 sc1\n.endif\n"
    "s_branch .Lhb%c[id]\n\t.subsection 0"
    : [x0] "+v"(x0), [x1] "+v"(x1), [x2] "+v"(x2), [x3] "+v"(x3), [x4] "+v"(x4), [x5] "+v"(x5), [x6] "+v"(x6), [x7] "+v"(x7), [x8] "+v"(x8), [x9] "+v"(x9), [x10] "+v"(x10), [x11] "+v"(x11), [x12] "+v"(x12)
    : [m] "s"(m), [xv] "v"(xv), [gx] "s"(gx), [a] "v"(x0[0]), [b] "v"(x0[1]), [c] "v"(x0[2]), [d] "v"(x0[3]), [id] "n"(ID), [w] "n"(W), [off] "n"(OFF)
    : "memory", "scc", "vcc", "s95", "v253", "v254", "v255");
}
template <int ID, int W, int OFF> DI void hole32(u64 m, unsigned xv, const char* gx, u32x4& x0, u32x4& x1, u32x4& x2, u32x4& x3, u32x4& x4, u32x4& x5, u32x4& x6, u32x4& x7, u32x4& x8, u32x4& x9, u32x4& x10, u32x4& x11, u32x4& x12, u32x4& x13) {
  asm volatile("s_cmp_lg_u64 %[m], 0\n\ts_cbranch_scc1 .Lhs%c[id]\n.Lhb%c[id]:\n\t.subsection 1\n.Lhs%c[id]:\n\ts_mov_b32 s95, " STR(SPINMAX) "\n"
    "v_add_u32 v254, %c[off], %[xv]\n\tv_add_u32 v253, %c[off]+8192, %[xv]\n.Lhp%c[id]:\n"
    ".if %c[w] > 0\nglobal_load_dwordx4 %[x0], v254, %[gx] offset:-4096 sc1\n.endif\n"
    ".if %c[w] > 1\nglobal_load_dwordx4 %[x1], v254, %[gx] offset:-3072 sc1\n.endif\n"
    ".if %c[w] > 2\nglobal_load_dwordx4 %[x2], v254, %[gx] offset:-2048 sc1\n.endif\n"
    ".if %c[w] > 3\nglobal_load_dwordx4 %[x3], v254, %[gx] offset:-1024 sc1\n.endif\n"
    ".if %c[w] > 4\nglobal_load_dwordx4 %[x4], v254, %[gx] offset:0 sc1\n.endif\n"
    ".if %c[w] > 5\nglobal_load_dwordx4 %[x5], v254, %[gx] offset:1024 sc1\n.endif\n"
    ".if %c[w] > 6\nglobal_load_dwordx4 %[x6], v254, %[gx] offset:2048 sc1\n.endif\n"
    ".if %c[w] > 7\nglobal_load_dwordx4 %[x7], v254, %[gx] offset:3072 sc1\n.endif\n"
    ".if %c[w] > 8\nglobal_load_dwordx4 %[x8], v253, %[gx] offset:-4096 sc1\n.endif\n"
    ".if %c[w] > 9\nglobal_load_dwordx4 %[x9], v253, %[gx] offset:-3072 sc1\n.endif\n"
    ".if %c[w] > 10\nglobal_load_dwordx4 %[x10], v253, %[gx] offset:-2048 sc1\n.endif\n"
    ".if %c[w] > 11\nglobal_load_dwordx4 %[x11], v253, %[gx] offset:-1024 sc1\n.endif\n"
    ".if %c[w] > 12\nglobal_load_dwordx4 %[x12], v253, %[gx] offset:0 sc1\n.endif\n"
    ".if %c[w] > 13\nglobal_load_dwordx4 %[x13], v253, %[gx] offset:1024 sc1\n.endif\n"
    "s_waitcnt vmcnt(%c[w]-1)\n\tv_max3_u32 v255, %[a], %[b], %[c]\n\tv_max_u32 v255, v255, %[d]\n\tv_cmp_eq_u32 vcc, -1, v255\n"
    "s_cbranch_vccz .Lhd%c[id]\n\ts_sleep " STR(RSLP) "\n\ts_sub_u32 s95, s95, 1\n\ts_cmp_lg_u32 s95, 0\n\ts_cbranch_scc1 .Lhp%c[id]\n.Lhd%c[id]:\n"
    ".if %c[w] <= 1\nglobal_load_dwordx4 %[x1], v254, %[gx] offset:-3072 sc1\n.endif\n"
    ".if %c[w] <= 2\nglobal_load_dwordx4 %[x2], v254, %[gx] offset:-2048 sc1\n.endif\n"
    ".if %c[w] <= 3\nglobal_load_dwordx4 %[x3], v254, %[gx] offset:-1024 sc1\n.endif\n"
    ".if %c[w] <= 4\nglobal_load_dwordx4 %[x4], v254, %[gx] offset:0 sc1\n.endif\n"
    ".if %c[w] <= 5\nglobal_load_dwordx4 %[x5], v254, %[gx] offset:1024 sc1\n.endif\n"
    ".if %c[w] <= 6\nglobal_load_dwordx4 %[x6], v254, %[gx] offset:2048 sc1\n.endif\n"
    ".if %c[w] <= 7\nglobal_load_dwordx4 %[x7], v254, %[gx] offset:3072 sc1\n.endif\n"
    ".if %c[w] <= 8\nglobal_load_dwordx4 %[x8], v253, %[gx] offset:-4096 sc1\n.endif\n"
    ".if %c[w] <= 9\nglobal_load_dwordx4 %[x9], v253, %[gx] offset:-3072 sc1\n.endif\n"
    ".if %c[w] <= 10\nglobal_load_dwordx4 %[x10], v253, %[gx] offset:-2048 sc1\n.endif\n"
    ".if %c[w] <= 11\nglobal_load_dwordx4 %[x11], v253, %[gx] offset:-1024 sc1\n.endif\n"
    ".if %c[w] <= 12\nglobal_load_dwordx4 %[x12], v253, %[gx] offset:0 sc1\n.endif\n"
    ".if %c[w] <= 13\nglobal_load_dwordx4 %[x13], v253, %[gx] offset:1024 sc1\n.endif\n"
    "s_branch .Lhb%c[id]\n\t.subsection 0"
    : [x0] "+v"(x0), [x1] "+v"(x1), [x2] "+v"(x2), [x3] "+v"(x3), [x4] "+v"(x4), [x5] "+v"(x5), [x6] "+v"(x6), [x7] "+v"(x7), [x8] "+v"(x8), [x9] "+v"(x9), [x10] "+v"(x10), [x11] "+v"(x11), [x12] "+v"(x12), [x13] "+v"(x13)
    : [m] "s"(m), [xv] "v"(xv), [gx] "s"(gx), [a] "v"(x0[0]), [b] "v"(x0[1]), [c] "v"(x0[2]), [d] "v"(x0[3]), [id] "n"(ID), [w] "n"(W), [off] "n"(OFF)
    : "memory", "scc", "vcc", "s95", "v253", "v254", "v255");
}
template <int ID, int W, int OFF> DI void hole32(u64 m, unsigned xv, const char* gx, u32x4& x0, u32x4& x1, u32x4& x2, u32x4& x3, u32x4& x4, u32x4& x5, u32x4& x6, u32x4& x7, u32x4& x8, u32x4& x9, u32x4& x10, u32x4& x11, u32x4& x12, u32x4& x13, u32x4& x14) {
  asm volatile("s_cmp_lg_u64 %[m], 0\n\ts_cbranch_scc1 .Lhs%c[id]\n.Lhb%c[id]:\n\t.subsection 1\n.Lhs%c[id]:\n\ts_mov_b32 s95, " STR(SPINMAX) "\n"
    "v_add_u32 v254, %c[off], %[xv]\n\tv_add_u32 v253, %c[off]+8192, %[xv]\n.Lhp%c[id]:\n"
    ".if %c[w] > 0\nglobal_load_dwordx4 %[x0], v254, %[gx] offset:-4096 sc1\n.endif\n"
    ".if %c[w] > 1\nglobal_load_dwordx4 %[x1], v254, %[gx] offset:-3072 sc1\n.endif\n"
    ".if %c[w] > 2\nglobal_load_dwordx4 %[x2], v254, %[gx] offset:-2048 sc1\n.endif\n"
    ".if %c[w] > 3\nglobal_load_dwordx4 %[x3], v254, %[gx] offset:-1024 sc1\n.endif\n"
    ".if %c[w] > 4\nglobal_load_dwordx4 %[x4], v254, %[gx] offset:0 sc1\n.endif\n"
    ".if %c[w] > 5\nglobal_load_dwordx4 %[x5], v254, %[gx] offset:1024 sc1\n.endif\n"
    ".if %c[w] > 6\nglobal_load_dwordx4 %[x6], v254, %[gx] offset:2048 sc1\n.endif\n"
    ".if %c[w] > 7\nglobal_load_dwordx4 %[x7], v254, %[gx] offset:3072 sc1\n.endif\n"
    ".if %c[w] > 8\nglobal_load_dwordx4 %[x8], v253, %[gx] offset:-4096 sc1\n.endif\n"
    ".if %c[w] > 9\nglobal_load_dwordx4 %[x9], v253, %[gx] offset:-3072 sc1\n.endif\n"
    ".if %c[w] > 10\nglobal_load_dwordx4 %[x10], v253, %[gx] offset:-2048 sc1\n.endif\n"
    ".if %c[w] > 11\nglobal_load_dwordx4 %[x11], v253, %[gx] offset:-1024 sc1\n.endif\n"
    ".if %c[w] > 12\nglobal_load_dwordx4 %[x12], v253, %[gx] offset:0 sc1\n.endif\n"
    ".if %c[w] > 13\nglobal_load_dwordx4 %[x13], v253, %[gx] offset:1024 sc1\n.endif\n"
    ".if %c[w] > 14\nglobal_load_dwordx4 %[x14], v253, %[gx] offset:2048 sc1\n.endif\n"
    "s_waitcnt vmcnt(%c[w]-1)\n\tv_max3_u32 v255, %[a], %[b], %[c]\n\tv_max_u32 v255, v255, %[d]\n\tv_cmp_eq_u32 vcc, -1, v255\n"
    "s_cbranch_vccz .Lhd%c[id]\n\ts_sleep " STR(RSLP) "\n\ts_sub_u32 s95, s95, 1\n\ts_cmp_lg_u32 s95, 0\n\ts_cbranch_scc1 .Lhp%c[id]\n.Lhd%c[id]:\n"
    ".if %c[w] <= 1\nglobal_load_dwordx4 %[x1], v254, %[gx] offset:-3072 sc1\n.endif\n"
    ".if %c[w] <= 2\nglobal_load_dwordx4 %[x2], v254, %[gx] offset:-2048 sc1\n.endif\n"
    ".if %c[w] <= 3\nglobal_load_dwordx4 %[x3], v254, %[gx] offset:-1024 sc1\n.endif\n"
    ".if %c[w] <= 4\nglobal_load_dwordx4 %[x4], v254, %[gx] offset:0 sc1\n.endif\n"
    ".if %c[w] <= 5\nglobal_load_dwordx4 %[x5], v254, %[gx] offset:1024 sc1\n.endif\n"
    ".if %c[w] <= 6\nglobal_load_dwordx4 %[x6], v254, %[gx] offset:2048 sc1\n.endif\n"
    ".if %c[w] <= 7\nglobal_load_dwordx4 %[x7], v254, %[gx] offset:3072 sc1\n.endif\n"
    ".if %c[w] <= 8\nglobal_load_dwordx4 %[x8], v253, %[gx] offset:-4096 sc1\n.endif\n"
    ".if %c[w] <= 9\nglobal_load_dwordx4 %[x9], v253, %[gx] offset:-3072 sc1\n.endif\n"
    ".if %c[w] <= 10\nglobal_load_dwordx4 %[x10], v253, %[gx] offset:-2048 sc1\n.endif\n"
    ".if %c[w] <= 11\nglobal_load_dwordx4 %[x11], v253, %[gx] offset:-1024 sc1\n.endif\n"
    ".if %c[w] <= 12\nglobal_load_dwordx4 %[x12], v253, %[gx] offset:0 sc1\n.endif\n"
    ".if %c[w] <= 13\nglobal_load_dwordx4 %[x13], v253, %[gx] offset:1024 sc1\n.endif\n"
    ".if %c[w] <= 14\nglobal_load_dwordx4 %[x14], v253, %[gx] offset:2048 sc1\n.endif\n"
    "s_branch .Lhb%c[id]\n\t.subsection 0"
    : [x0] "+v"(x0), [x1] "+v"(x1), [x2] "+v"(x2), [x3] "+v"(x3), [x4] "+v"(x4), [x5] "+v"(x5), [x6] "+v"(x6), [x7] "+v"(x7), [x8] "+v"(x8), [x9] "+v"(x9), [x10] "+v"(x10), [x11] "+v"(x11), [x12] "+v"(x12), [x13] "+v"(x13), [x14] "+v"(x14)
    : [m] "s"(m), [xv] "v"(xv), [gx] "s"(gx), [a] "v"(x0[0]), [b] "v"(x0[1]), [c] "v"(x0[2]), [d] "v"(x0[3]), [id] "n"(ID), [w] "n"(W), [off] "n"(OFF)
    : "memory", "scc", "vcc", "s95", "v253", "v254", "v255");
}
template <int ID, int W, int OFF> DI void hole32(u64 m, unsigned xv, const char* gx, u32x4& x0, u32x4& x1, u32x4& x2, u32x4& x3, u32x4& x4, u32x4& x5, u32x4& x6, u32x4& x7, u32x4& x8, u32x4& x9, u32x4& x10, u32x4& x11, u32x4& x12, u32x4& x13, u32x4& x14, u32x4& x15) {
  asm volatile("s_cmp_lg_u64 %[m], 0\n\ts_cbranch_scc1 .Lhs%c[id]\n.Lhb%c[id]:\n\t.subsection 1\n.Lhs%c[id]:\n\ts_mov_b32 s95, " STR(SPINMAX) "\n"
    "v_add_u32 v254, %c[off], %[xv]\n\tv_add_u32 v253, %c[off]+8192, %[xv]\n.Lhp%c[id]:\n"
    ".if %c[w] > 0\nglobal_load_dwordx4 %[x0], v254, %[gx] offset:-4096 sc1\n.endif\n"
    ".if %c[w] > 1\nglobal_load_dwordx4 %[x1], v254, %[gx] offset:-3072 sc1\n.endif\n"
    ".if %c[w] > 2\nglobal_load_dwordx4 %[x2], v254, %[gx] offset:-2048 sc1\n.endif\n"
    ".if %c[w] > 3\nglobal_load_dwordx4 %[x3], v254, %[gx] offset:-1024 sc1\n.endif\n"
    ".if %c[w] > 4\nglobal_load_dwordx4 %[x4], v254, %[gx] offset:0 sc1\n.endif\n"
    ".if %c[w] > 5\nglobal_load_dwordx4 %[x5], v254, %[gx] offset:1024 sc1\n.endif\n"
    ".if %c[w] > 6\nglobal_load_dwordx4 %[x6], v254, %[gx] offset:2048 sc1\n.endif\n"
    ".if %c[w] > 7\nglobal_load_dwordx4 %[x7], v254, %[gx] offset:3072 sc1\n.endif\n"
    ".if %c[w] > 8\nglobal_load_dwordx4 %[x8], v253, %[gx] offset:-4096 sc1\n.endif\n"
    ".if %c[w] > 9\nglobal_load_dwordx4 %[x9], v253, %[gx] offset:-3072 sc1\n.endif\n"
    ".if %c[w] > 10\nglobal_load_dwordx4 %[x10], v253, %[gx] offset:-2048 sc1\n.endif\n"
    ".if %c[w] > 11\nglobal_load_dwordx4 %[x11], v253, %[gx] offset:-1024 sc1\n.endif\n"
    ".if %c[w] > 12\nglobal_load_dwordx4 %[x12], v253, %[gx] offset:0 sc1\n.endif\n"
    ".if %c[w] > 13\nglobal_load_dwordx4 %[x13], v253, %[gx] offset:1024 sc1\n.endif\n"
    ".if %c[w] > 14\nglobal_load_dwordx4 %[x14], v253, %[gx] offset:2048 sc1\n.endif\n"
    ".if %c[w] > 15\nglobal_load_dwordx4 %[x15], v253, %[gx] offset:3072 sc1\n.endif\n"
    "s_waitcnt vmcnt(%c[w]-1)\n\tv_max3_u32 v255, %[a], %[b], %[c]\n\tv_max_u32 v255, v255, %[d]\n\tv_cmp_eq_u32 vcc, -1, v255\n"
    "s_cbranch_vccz .Lhd%c[id]\n\ts_sleep " STR(RSLP) "\n\ts_sub_u32 s95, s95, 1\n\ts_cmp_lg_u32 s95, 0\n\ts_cbranch_scc1 .Lhp%c[id]\n.Lhd%c[id]:\n"
    ".if %c[w] <= 1\nglobal_load_dwordx4 %[x1], v254, %[gx] offset:-3072 sc1\n.endif\n"
    ".if %c[w] <= 2\nglobal_load_dwordx4 %[x2], v254, %[gx] offset:-2048 sc1\n.endif\n"
    ".if %c[w] <= 3\nglobal_load_dwordx4 %[x3], v254, %[gx] offset:-1024 sc1\n.endif\n"
    ".if %c[w] <= 4\nglobal_load_dwordx4 %[x4], v254, %[gx] offset:0 sc1\n.endif\n"
    ".if %c[w] <= 5\nglobal_load_dwordx4 %[x5], v254, %[gx] offset:1024 sc1\n.endif\n"
    ".if %c[w] <= 6\nglobal_load_dwordx4 %[x6], v254, %[gx] offset:2048 sc1\n.endif\n"
    ".if %c[w] <= 7\nglobal_load_dwordx4 %[x7], v254, %[gx] offset:3072 sc1\n.endif\n"
    ".if %c[w] <= 8\nglobal_load_dwordx4 %[x8], v253, %[gx] offset:-4096 sc1\n.endif\n"
    ".if %c[w] <= 9\nglobal_load_dwordx4 %[x9], v253, %[gx] offset:-3072 sc1\n.endif\n"
    ".if %c[w] <= 10\nglobal_load_dwordx4 %[x10], v253, %[gx] offset:-2048 sc1\n.endif\n"
    ".if %c[w] <= 11\nglobal_load_dwordx4 %[x11], v253, %[gx] offset:-1024 sc1\n.endif\n"
    ".if %c[w] <= 12\nglobal_load_dwordx4 %[x12], v253, %[gx] offset:0 sc1\n.endif\n"
    ".if %c[w] <= 13\nglobal_load_dwordx4 %[x13], v253, %[gx] offset:1024 sc1\n.endif\n"
    ".if %c[w] <= 14\nglobal_load_dwordx4 %[x14], v253, %[gx] offset:2048 sc1\n.endif\n"
    ".if %c[w] <= 15\nglobal_load_dwordx4 %[x15], v253, %[gx] offset:3072 sc1\n.endif\n"
    "s_branch .Lhb%c[id]\n\t.subsection 0"
    : [x0] "+v"(x0), [x1] "+v"(x1), [x2] "+v"(x2), [x3] "+v"(x3), [x4] "+v"(x4), [x5] "+v"(x5), [x6] "+v"(x6), [x7] "+v"(x7), [x8] "+v"(x8), [x9] "+v"(x9), [x10] "+v"(x10), [x11] "+v"(x11), [x12] "+v"(x12), [x13] "+v"(x13), [x14] "+v"(x14), [x15] "+v"(x15)
    : [m] "s"(m), [xv] "v"(xv), [gx] "s"(gx), [a] "v"(x0[0]), [b] "v"(x0[1]), [c] "v"(x0[2]), [d] "v"(x0[3]), [id] "n"(ID), [w] "n"(W), [off] "n"(OFF)
    : "memory", "scc", "vcc", "s95", "v253", "v254", "v255");
}
template <int ID, int PAR, int OFF> DI void hole16a(u64 m, unsigned xv0, unsigned xv1, const char* gx, u32x4& x0, u32x4& y0) {
  asm volatile("s_cmp_lg_u64 %[m], 0\n\ts_cbranch_scc1 .Lhs%c[id]\n.Lhb%c[id]:\n\t.subsection 1\n.Lhs%c[id]:\n\ts_mov_b32 s95, " STR(SPINMAX) "\n"
    ".Lhp%c[id]:\n\tv_add_u32 v254, %c[off], %[xv0]\n\tv_add_u32 v253, %c[off], %[xv1]\n"
    "global_load_dwordx4 %[x0], v254, %[gx] offset:((%c[par]+0)&1)*2048 sc1\nglobal_load_dwordx4 %[y0], v253, %[gx] offset:((%c[par]+0)&1)*2048 sc1\n"
    "s_waitcnt vmcnt(0)\n\tv_max3_u32 v255, %[a], %[b], %[c]\n\tv_max3_u32 v255, v255, %[d], %[e]\n\tv_max3_u32 v255, v255, %[f], %[g]\n\tv_max_u32 v255, v255, %[h]\n\tv_cmp_eq_u32 vcc, -1, v255\n"
    "s_cbranch_vccz .Lhd%c[id]\n\ts_sleep " STR(RSLP) "\n\ts_sub_u32 s95, s95, 1\n\ts_cmp_lg_u32 s95, 0\n\ts_cbranch_scc1 .Lhp%c[id]\n.Lhd%c[id]:\n"
    ".subsection 0"
    : [x0] "+v"(x0), [y0] "+v"(y0)
    : [m] "s"(m), [xv0] "v"(xv0), [xv1] "v"(xv1), [gx] "s"(gx), [a] "v"(x0[0]), [b] "v"(x0[1]), [c] "v"(x0[2]), [d] "v"(x0[3]),
      [e] "v"(y0[0]), [f] "v"(y0[1]), [g] "v"(y0[2]), [h] "v"(y0[3]), [id] "n"(ID), [par] "n"(PAR), [off] "n"(OFF)
    : "memory", "scc", "vcc", "s95", "v253", "v254", "v255");
}
template <int ID, int PAR, int OFF> DI void hole16a(u64 m, unsigned xv0, unsigned xv1, const char* gx, u32x4& x0, u32x4& y0, u32x4& x1, u32x4& y1) {
  asm volatile("s_cmp_lg_u64 %[m], 0\n\ts_cbranch_scc1 .Lhs%c[id]\n.Lhb%c[id]:\n\t.subsection 1\n.Lhs%c[id]:\n\ts_mov_b32 s95, " STR(SPINMAX) "\n"
    ".Lhp%c[id]:\n\tv_add_u32 v254, %c[off], %[xv0]\n\tv_add_u32 v253, %c[off], %[xv1]\n"
    "global_load_dwordx4 %[x0], v254, %[gx] offset:((%c[par]+0)&1)*2048 sc1\nglobal_load_dwordx4 %[y0], v253, %[gx] offset:((%c[par]+0)&1)*2048 sc1\n"
    ".if ((%c[par]+1)&1) == 0\nv_add_u32 v254, 0x2000, v254\nv_add_u32 v253, 0x2000, v253\n.endif\nglobal_load_dwordx4 %[x1], v254, %[gx] offset:((%c[par]+1)&1)*2048 sc1\nglobal_load_dwordx4 %[y1], v253, %[gx] offset:((%c[par]+1)&1)*2048 sc1\n"
    "s_waitcnt vmcnt(2)\n\tv_max3_u32 v255, %[a], %[b], %[c]\n\tv_max3_u32 v255, v255, %[d], %[e]\n\tv_max3_u32 v255, v255, %[f], %[g]\n\tv_max_u32 v255, v255, %[h]\n\tv_cmp_eq_u32 vcc, -1, v255\n"
    "s_cbranch_vccz .Lhd%c[id]\n\ts_sleep " STR(RSLP) "\n\ts_sub_u32 s95, s95, 1\n\ts_cmp_lg_u32 s95, 0\n\ts_cbranch_scc1 .Lhp%c[id]\n.Lhd%c[id]:\n"
    ".subsection 0"
    : [x0] "+v"(x0), [y0] "+v"(y0), [x1] "+v"(x1), [y1] "+v"(y1)
    : [m] "s"(m), [xv0] "v"(xv0), [xv1] "v"(xv1), [gx] "s"(gx), [a] "v"(x0[0]), [b] "v"(x0[1]), [c] "v"(x0[2]), [d] "v"(x0[3]),
      [e] "v"(y0[0]), [f] "v"(y0[1]), [g] "v"(y0[2]), [h] "v"(y0[3]), [id] "n"(ID), [par] "n"(PAR), [off] "n"(OFF)
    : "memory", "scc", "vcc", "s95", "v253", "v254", "v255");
}
template <int ID, int PAR, int OFF> DI void hole16a(u64 m, unsigned xv0, unsigned xv1, const char* gx, u32x4& x0, u32x4& y0, u32x4& x1, u32x4& y1, u32x4& x2, u32x4& y2) {
  asm volatile("s_cmp_lg_u64 %[m], 0\n\ts_cbranch_scc1 .Lhs%c[id]\n.Lhb%c[id]:\n\t.subsection 1\n.Lhs%c[id]:\n\ts_mov_b32 s95, " STR(SPINMAX) "\n"
    ".Lhp%c[id]:\n\tv_add_u32 v254, %c[off], %[xv0]\n\tv_add_u32 v253, %c[off], %[xv1]\n"
    "global_load_dwordx4 %[x0], v254, %[gx] offset:((%c[par]+0)&1)*2048 sc1\nglobal_load_dwordx4 %[y0], v253, %[gx] offset:((%c[par]+0)&1)*2048 sc1\n"
    ".if ((%c[par]+1)&1) == 0\nv_add_u32 v254, 0x2000, v254\nv_add_u32 v253, 0x2000, v253\n.endif\nglobal_load_dwordx4 %[x1], v254, %[gx] offset:((%c[par]+1)&1)*2048 sc1\nglobal_load_dwordx4 %[y1], v253, %[gx] offset:((%c[par]+1)&1)*2048 sc1\n"
    ".if ((%c[par]+2)&1) == 0\nv_add_u32 v254, 0x2000, v254\nv_add_u32 v253, 0x2000, v253\n.endif\nglobal_load_dwordx4 %[x2], v254, %[gx] offset:((%c[par]+2)&1)*2048 sc1\nglobal_load_dwordx4 %[y2], v253, %[gx] offset:((%c[par]+2)&1)*2048 sc1\n"
    "s_waitcnt vmcnt(4)\n\tv_max3_u32 v255, %[a], %[b], %[c]\n\tv_max3_u32 v255, v255, %[d], %[e]\n\tv_max3_u32 v255, v255, %[f], %[g]\n\tv_max_u32 v255, v255, %[h]\n\tv_cmp_eq_u32 vcc, -1, v255\n"
    "s_cbranch_vccz .Lhd%c[id]\n\ts_sleep " STR(RSLP) "\n\ts_sub_u32 s95, s95, 1\n\ts_cmp_lg_u32 s95, 0\n\ts_cbranch_scc1 .Lhp%c[id]\n.Lhd%c[id]:\n"
    ".subsection 0"
    : [x0] "+v"(x0), [y0] "+v"(y0), [x1] "+v"(x1), [y1] "+v"(y1), [x2] "+v"(x2), [y2] "+v"(y2)
    : [m] "s"(m), [xv0] "v"(xv0), [xv1] "v"(xv1), [gx] "s"(gx), [a] "v"(x0[0]), [b] "v"(x0[1]), [c] "v"(x0[2]), [d] "v"(x0[3]),
      [e] "v"(y0[0]), [f] "v"(y0[1]), [g] "v"(y0[2]), [h] "v"(y0[3]), [id] "n"(ID), [par] "n"(PAR), [off] "n"(OFF)
    : "memory", "scc", "vcc", "s95", "v253", "v254", "v255");
}
template <int ID, int PAR, int OFF> DI void hole16a(u64 m, unsigned xv0, unsigned xv1, const char* gx, u32x4& x0, u32x4& y0, u32x4& x1, u32x4& y1, u32x4& x2, u32x4& y2, u32x4& x3, u32x4& y3) {
  asm volatile("s_cmp_lg_u64 %[m], 0\n\ts_cbranch_scc1 .Lhs%c[id]\n.Lhb%c[id]:\n\t.subsection 1\n.Lhs%c[id]:\n\ts_mov_b32 s95, " STR(SPINMAX) "\n"
    ".Lhp%c[id]:\n\tv_add_u32 v254, %c[off], %[xv0]\n\tv_add_u32 v253, %c[off], %[xv1]\n"
    "global_load_dwordx4 %[x0], v254, %[gx] offset:((%c[par]+0)&1)*2048 sc1\nglobal_load_dwordx4 %[y0], v253, %[gx] offset:((%c[par]+0)&1)*2048 sc1\n"
    ".if ((%c[par]+1)&1) == 0\nv_add_u32 v254, 0x2000, v254\nv_add_u32 v253, 0x2000, v253\n.endif\nglobal_load_dwordx4 %[x1], v254, %[gx] offset:((%c[par]+1)&1)*2048 sc1\nglobal_load_dwordx4 %[y1], v253, %[gx] offset:((%c[par]+1)&1)*2048 sc1\n"
    ".if ((%c[par]+2)&1) == 0\nv_add_u32 v254, 0x2000, v254\nv_add_u32 v253, 0x2000, v253\n.endif\nglobal_load_dwordx4 %[x2], v254, %[gx] offset:((%c[par]+2)&1)*2048 sc1\nglobal_load_dwordx4 %[y2], v253, %[gx] offset:((%c[par]+2)&1)*2048 sc1\n"
    ".if ((%c[par]+3)&1) == 0\nv_add_u32 v254, 0x2000, v254\nv_add_u32 v253, 0x2000, v253\n.endif\nglobal_load_dwordx4 %[x3], v254, %[gx] offset:((%c[par]+3)&1)*2048 sc1\nglobal_load_dwordx4 %[y3], v253, %[gx] offset:((%c[par]+3)&1)*2048 sc1\n"
    "s_waitcnt vmcnt(6)\n\tv_max3_u32 v255, %[a], %[b], %[c]\n\tv_max3_u32 v255, v255, %[d], %[e]\n\tv_max3_u32 v255, v255, %[f], %[g]\n\tv_max_u32 v255, v255, %[h]\n\tv_cmp_eq_u32 vcc, -1, v255\n"
    "s_cbranch_vccz .Lhd%c[id]\n\ts_sleep " STR(RSLP) "\n\ts_sub_u32 s95, s95, 1\n\ts_cmp_lg_u32 s95, 0\n\ts_cbranch_scc1 .Lhp%c[id]\n.Lhd%c[id]:\n"
    ".subsection 0"
    : [x0] "+v"(x0), [y0] "+v"(y0), [x1] "+v"(x1), [y1] "+v"(y1), [x2] "+v"(x2), [y2] "+v"(y2), [x3] "+v"(x3), [y3] "+v"(y3)
    : [m] "s"(m), [xv0] "v"(xv0), [xv1] "v"(xv1), [gx] "s"(gx), [a] "v"(x0[0]), [b] "v"(x0[1]), [c] "v"(x0[2]), [d] "v"(x0[3]),
      [e] "v"(y0[0]), [f] "v"(y0[1]), [g] "v"(y0[2]), [h] "v"(y0[3]), [id] "n"(ID), [par] "n"(PAR), [off] "n"(OFF)
    : "memory", "scc", "vcc", "s95", "v253", "v254", "v255");
}
template <int ID, int PAR, int OFF> DI void hole16a(u64 m, unsigned xv0, unsigned xv1, const char* gx, u32x4& x0, u32x4& y0, u32x4& x1, u32x4& y1, u32x4& x2, u32x4& y2, u32x4& x3, u32x4& y3, u32x4& x4, u32x4& y4) {
  asm volatile("s_cmp_lg_u64 %[m], 0\n\ts_cbranch_scc1 .Lhs%c[id]\n.Lhb%c[id]:\n\t.subsection 1\n.Lhs%c[id]:\n\ts_mov_b32 s95, " STR(SPINMAX) "\n"
    ".Lhp%c[id]:\n\tv_add_u32 v254, %c[off], %[xv0]\n\tv_add_u32 v253, %c[off], %[xv1]\n"
    "global_load_dwordx4 %[x0], v254, %[gx] offset:((%c[par]+0)&1)*2048 sc1\nglobal_load_dwordx4 %[y0], v253, %[gx] offset:((%c[par]+0)&1)*2048 sc1\n"
    ".if ((%c[par]+1)&1) == 0\nv_add_u32 v254, 0x2000, v254\nv_add_u32 v253, 0x2000, v253\n.endif\nglobal_load_dwordx4 %[x1], v254, %[gx] offset:((%c[par]+1)&1)*2048 sc1\nglobal_load_dwordx4 %[y1], v253, %[gx] offset:((%c[par]+1)&1)*2048 sc1\n"
    ".if ((%c[par]+2)&1) == 0\nv_add_u32 v254, 0x2000, v254\nv_add_u32 v253, 0x2000, v253\n.endif\nglobal_load_dwordx4 %[x2], v254, %[gx] offset:((%c[par]+2)&1)*2048 sc1\nglobal_load_dwordx4 %[y2], v253, %[gx] offset:((%c[par]+2)&1)*2048 sc1\n"
    ".if ((%c[par]+3)&1) == 0\nv_add_u32 v254, 0x2000, v254\nv_add_u32 v253, 0x2000, v253\n.endif\nglobal_load_dwordx4 %[x3], v254, %[gx] offset:((%c[par]+3)&1)*2048 sc1\nglobal_load_dwordx4 %[y3], v253, %[gx] offset:((%c[par]+3)&1)*2048 sc1\n"
    ".if ((%c[par]+4)&1) == 0\nv_add_u32 v254, 0x2000, v254\nv_add_u32 v253, 0x2000, v253\n.endif\nglobal_load_dwordx4 %[x4], v254, %[gx] offset:((%c[par]+4)&1)*2048 sc1\nglobal_load_dwordx4 %[y4], v253, %[gx] offset:((%c[par]+4)&1)*2048 sc1\n"
    "s_waitcnt vmcnt(8)\n\tv_max3_u32 v255, %[a], %[b], %[c]\n\tv_max3_u32 v255, v255, %[d], %[e]\n\tv_max3_u32 v255, v255, %[f], %[g]\n\tv_max_u32 v255, v255, %[h]\n\tv_cmp_eq_u32 vcc, -1, v255\n"
    "s_cbranch_vccz .Lhd%c[id]\n\ts_sleep " STR(RSLP) "\n\ts_sub_u32 s95, s95, 1\n\ts_cmp_lg_u32 s95, 0\n\ts_cbranch_scc1 .Lhp%c[id]\n.Lhd%c[id]:\n"
    ".subsection 0"
    : [x0] "+v"(x0), [y0] "+v"(y0), [x1] "+v"(x1), [y1] "+v"(y1), [x2] "+v"(x2), [y2] "+v"(y2), [x3] "+v"(x3), [y3] "+v"(y3), [x4] "+v"(x4), [y4] "+v"(y4)
    : [m] "s"(m), [xv0] "v"(xv0), [xv1] "v"(xv1), [gx] "s"(gx), [a] "v"(x0[0]), [b] "v"(x0[1]), [c] "v"(x0[2]), [d] "v"(x0[3]),
      [e] "v"(y0[0]), [f] "v"(y0[1]), [g] "v"(y0[2]), [h] "v"(y0[3]), [id] "n"(ID), [par] "n"(PAR), [off] "n"(OFF)
    : "memory", "scc", "vcc", "s95", "v253", "v254", "v255");
}
template <int ID, int PAR, int OFF> DI void hole16a(u64 m, unsigned xv0, unsigned xv1, const char* gx, u32x4& x0, u32x4& y0, u32x4& x1, u32x4& y1, u32x4& x2, u32x4& y2, u32x4& x3, u32x4& y3, u32x4& x4, u32x4& y4, u32x4& x5, u32x4& y5) {
  asm volatile("s_cmp_lg_u64 %[m], 0\n\ts_cbranch_scc1 .Lhs%c[id]\n.Lhb%c[id]:\n\t.subsection 1\n.Lhs%c[id]:\n\ts_mov_b32 s95, " STR(SPINMAX) "\n"
    ".Lhp%c[id]:\n\tv_add_u32 v254, %c[off], %[xv0]\n\tv_add_u32 v253, %c[off], %[xv1]\n"
    "global_load_dwordx4 %[x0], v254, %[gx] offset:((%c[par]+0)&1)*2048 sc1\nglobal_load_dwordx4 %[y0], v253, %[gx] offset:((%c[par]+0)&1)*2048 sc1\n"
    ".if ((%c[par]+1)&1) == 0\nv_add_u32 v254, 0x2000, v254\nv_add_u32 v253, 0x2000, v253\n.endif\nglobal_load_dwordx4 %[x1], v254, %[gx] offset:((%c[par]+1)&1)*2048 sc1\nglobal_load_dwordx4 %[y1], v253, %[gx] offset:((%c[par]+1)&1)*2048 sc1\n"
    ".if ((%c[par]+2)&1) == 0\nv_add_u32 v254, 0x2000, v254\nv_add_u32 v253, 0x2000, v253\n.endif\nglobal_load_dwordx4 %[x2], v254, %[gx] offset:((%c[par]+2)&1)*2048 sc1\nglobal_load_dwordx4 %[y2], v253, %[gx] offset:((%c[par]+2)&1)*2048 sc1\n"
    ".if ((%c[par]+3)&1) == 0\nv_add_u32 v254, 0x2000, v254\nv_add_u32 v253, 0x2000, v253\n.endif\nglobal_load_dwordx4 %[x3], v254, %[gx] offset:((%c[par]+3)&1)*2048 sc1\nglobal_load_dwordx4 %[y3], v253, %[gx] offset:((%c[par]+3)&1)*2048 sc1\n"
    ".if ((%c[par]+4)&1) == 0\nv_add_u32 v254, 0x2000, v254\nv_add_u32 v253, 0x2000, v253\n.endif\nglobal_load_dwordx4 %[x4], v254, %[gx] offset:((%c[par]+4)&1)*2048 sc1\nglobal_load_dwordx4 %[y4], v253, %[gx] offset:((%c[par]+4)&1)*2048 sc1\n"
    ".if ((%c[par]+5)&1) == 0\nv_add_u32 v254, 0x2000, v254\nv_add_u32 v253, 0x2000, v253\n.endif\nglobal_load_dwordx4 %[x5], v254, %[gx] offset:((%c[par]+5)&1)*2048 sc1\nglobal_load_dwordx4 %[y5], v253, %[gx] offset:((%c[par]+5)&1)*2048 sc1\n"
    "s_waitcnt vmcnt(10)\n\tv_max3_u32 v255, %[a], %[b], %[c]\n\tv_max3_u32 v255, v255, %[d], %[e]\n\tv_max3_u32 v255, v255, %[f], %[g]\n\tv_max_u32 v255, v255, %[h]\n\tv_cmp_eq_u32 vcc, -1, v255\n"
    "s_cbranch_vccz .Lhd%c[id]\n\ts_sleep " STR(RSLP) "\n\ts_sub_u32 s95, s95, 1\n\ts_cmp_lg_u32 s95, 0\n\ts_cbranch_scc1 .Lhp%c[id]\n.Lhd%c[id]:\n"
    ".subsection 0"
    : [x0] "+v"(x0), [y0] "+v"(y0), [x1] "+v"(x1), [y1] "+v"(y1), [x2] "+v"(x2), [y2] "+v"(y2), [x3] "+v"(x3), [y3] "+v"(y3), [x4] "+v"(x4), [y4] "+v"(y4), [x5] "+v"(x5), [y5] "+v"(y5)
    : [m] "s"(m), [xv0] "v"(xv0), [xv1] "v"(xv1), [gx] "s"(gx), [a] "v"(x0[0]), [b] "v"(x0[1]), [c] "v"(x0[2]), [d] "v"(x0[3]),
      [e] "v"(y0[0]), [f] "v"(y0[1]), [g] "v"(y0[2]), [h] "v"(y0[3]), [id] "n"(ID), [par] "n"(PAR), [off] "n"(OFF)
    : "memory", "scc", "vcc", "s95", "v253", "v254", "v255");
}
template <int ID, int PAR, int OFF> DI void hole16a(u64 m, unsigned xv0, unsigned xv1, const char* gx, u32x4& x0, u32x4& y0, u32x4& x1, u32x4& y1, u32x4& x2, u32x4& y2, u32x4& x3, u32x4& y3, u32x4& x4, u32x4& y4, u32x4& x5, u32x4& y5, u32x4& x6, u32x4& y6) {
  asm volatile("s_cmp_lg_u64 %[m], 0\n\ts_cbranch_scc1 .Lhs%c[id]\n.Lhb%c[id]:\n\t.subsection 1\n.Lhs%c[id]:\n\ts_mov_b32 s95, " STR(SPINMAX) "\n"
    ".Lhp%c[id]:\n\tv_add_u32 v254, %c[off], %[xv0]\n\tv_add_u32 v253, %c[off], %[xv1]\n"
    "global_load_dwordx4 %[x0], v254, %[gx] offset:((%c[par]+0)&1)*2048 sc1\nglobal_load_dwordx4 %[y0], v253, %[gx] offset:((%c[par]+0)&1)*2048 sc1\n"
    ".if ((%c[par]+1)&1) == 0\nv_add_u32 v254, 0x2000, v254\nv_add_u32 v253, 0x2000, v253\n.endif\nglobal_load_dwordx4 %[x1], v254, %[gx] offset:((%c[par]+1)&1)*2048 sc1\nglobal_load_dwordx4 %[y1], v253, %[gx] offset:((%c[par]+1)&1)*2048 sc1\n"
    ".if ((%c[par]+2)&1) == 0\nv_add_u32 v254, 0x2000, v254\nv_add_u32 v253, 0x2000, v253\n.endif\nglobal_load_dwordx4 %[x2], v254, %[gx] offset:((%c[par]+2)&1)*2048 sc1\nglobal_load_dwordx4 %[y2], v253, %[gx] offset:((%c[par]+2)&1)*2048 sc1\n"
    ".if ((%c[par]+3)&1) == 0\nv_add_u32 v254, 0x2000, v254\nv_add_u32 v253, 0x2000, v253\n.endif\nglobal_load_dwordx4 %[x3], v254, %[gx] offset:((%c[par]+3)&1)*2048 sc1\nglobal_load_dwordx4 %[y3], v253, %[gx] offset:((%c[par]+3)&1)*2048 sc1\n"
    ".if ((%c[par]+4)&1) == 0\nv_add_u32 v254, 0x2000, v254\nv_add_u32 v253, 0x2000, v253\n.endif\nglobal_load_dwordx4 %[x4], v254, %[gx] offset:((%c[par]+4)&1)*2048 sc1\nglobal_load_dwordx4 %[y4], v253, %[gx] offset:((%c[par]+4)&1)*2048 sc1\n"
    ".if ((%c[par]+5)&1) == 0\nv_add_u32 v254, 0x2000, v254\nv_add_u32 v253, 0x2000, v253\n.endif\nglobal_load_dwordx4 %[x5], v254, %[gx] offset:((%c[par]+5)&1)*2048 sc1\nglobal_load_dwordx4 %[y5], v253, %[gx] offset:((%c[par]+5)&1)*2048 sc1\n"
    ".if ((%c[par]+6)&1) == 0\nv_add_u32 v254, 0x2000, v254\nv_add_u32 v253, 0x2000, v253\n.endif\nglobal_load_dwordx4 %[x6], v254, %[gx] offset:((%c[par]+6)&1)*2048 sc1\nglobal_load_dwordx4 %[y6], v253, %[gx] offset:((%c[par]+6)&1)*2048 sc1\n"
    "s_waitcnt vmcnt(12)\n\tv_max3_u32 v255, %[a], %[b], %[c]\n\tv_max3_u32 v255, v255, %[d], %[e]\n\tv_max3_u32 v255, v255, %[f], %[g]\n\tv_max_u32 v255, v255, %[h]\n\tv_cmp_eq_u32 vcc, -1, v255\n"
    "s_cbranch_vccz .Lhd%c[id]\n\ts_sleep " STR(RSLP) "\n\ts_sub_u32 s95, s95, 1\n\ts_cmp_lg_u32 s95, 0\n\ts_cbranch_scc1 .Lhp%c[id]\n.Lhd%c[id]:\n"
    ".subsection 0"
    : [x0] "+v"(x0), [y0] "+v"(y0), [x1] "+v"(x1), [y1] "+v"(y1), [x2] "+v"(x2), [y2] "+v"(y2), [x3] "+v"(x3), [y3] "+v"(y3), [x4] "+v"(x4), [y4] "+v"(y4), [x5] "+v"(x5), [y5] "+v"(y5), [x6] "+v"(x6), [y6] "+v"(y6)
    : [m] "s"(m), [xv0] "v"(xv0), [xv1] "v"(xv1), [gx] "s"(gx), [a] "v"(x0[0]), [b] "v"(x0[1]), [c] "v"(x0[2]), [d] "v"(x0[3]),
      [e] "v"(y0[0]), [f] "v"(y0[1]), [g] "v"(y0[2]), [h] "v"(y0[3]), [id] "n"(ID), [par] "n"(PAR), [off] "n"(OFF)
    : "memory", "scc", "vcc", "s95", "v253", "v254", "v255");
}
template <int ID, int PAR, int OFF> DI void hole16a(u64 m, unsigned xv0, unsigned xv1, const char* gx, u32x4& x0, u32x4& y0, u32x4& x1, u32x4& y1, u32x4& x2, u32x4& y2, u32x4& x3, u32x4& y3, u32x4& x4, u32x4& y4, u32x4& x5, u32x4& y5, u32x4& x6, u32x4& y6, u32x4& x7, u32x4& y7) {
  asm volatile("s_cmp_lg_u64 %[m], 0\n\ts_cbranch_scc1 .Lhs%c[id]\n.Lhb%c[id]:\n\t.subsection 1\n.Lhs%c[id]:\n\ts_mov_b32 s95, " STR(SPINMAX) "\n"
    ".Lhp%c[id]:\n\tv_add_u32 v254, %c[off], %[xv0]\n\tv_add_u32 v253, %c[off], %[xv1]\n"
    "global_load_dwordx4 %[x0], v254, %[gx] offset:((%c[par]+0)&1)*2048 sc1\nglobal_load_dwordx4 %[y0], v253, %[gx] offset:((%c[par]+0)&1)*2048 sc1\n"
    ".if ((%c[par]+1)&1) == 0\nv_add_u32 v254, 0x2000, v254\nv_add_u32 v253, 0x2000, v253\n.endif\nglobal_load_dwordx4 %[x1], v254, %[gx] offset:((%c[par]+1)&1)*2048 sc1\nglobal_load_dwordx4 %[y1], v253, %[gx] offset:((%c[par]+1)&1)*2048 sc1\n"
    ".if ((%c[par]+2)&1) == 0\nv_add_u32 v254, 0x2000, v254\nv_add_u32 v253, 0x2000, v253\n.endif\nglobal_load_dwordx4 %[x2], v254, %[gx] offset:((%c[par]+2)&1)*2048 sc1\nglobal_load_dwordx4 %[y2], v253, %[gx] offset:((%c[par]+2)&1)*2048 sc1\n"
    ".if ((%c[par]+3)&1) == 0\nv_add_u32 v254, 0x2000, v254\nv_add_u32 v253, 0x2000, v253\n.endif\nglobal_load_dwordx4 %[x3], v254, %[gx] offset:((%c[par]+3)&1)*2048 sc1\nglobal_load_dwordx4 %[y3], v253, %[gx] offset:((%c[par]+3)&1)*2048 sc1\n"
    ".if ((%c[par]+4)&1) == 0\nv_add_u32 v254, 0x2000, v254\nv_add_u32 v253, 0x2000, v253\n.endif\nglobal_load_dwordx4 %[x4], v254, %[gx] offset:((%c[par]+4)&1)*2048 sc1\nglobal_load_dwordx4 %[y4], v253, %[gx] offset:((%c[par]+4)&1)*2048 sc1\n"
    ".if ((%c[par]+5)&1) == 0\nv_add_u32 v254, 0x2000, v254\nv_add_u32 v253, 0x2000, v253\n.endif\nglobal_load_dwordx4 %[x5], v254, %[gx] offset:((%c[par]+5)&1)*2048 sc1\nglobal_load_dwordx4 %[y5], v253, %[gx] offset:((%c[par]+5)&1)*2048 sc1\n"
    ".if ((%c[par]+6)&1) == 0\nv_add_u32 v254, 0x2000, v254\nv_add_u32 v253, 0x2000, v253\n.endif\nglobal_load_dwordx4 %[x6], v254, %[gx] offset:((%c[par]+6)&1)*2048 sc1\nglobal_load_dwordx4 %[y6], v253, %[gx] offset:((%c[par]+6)&1)*2048 sc1\n"
    ".if ((%c[par]+7)&1) == 0\nv_add_u32 v254, 0x2000, v254\nv_add_u32 v253, 0x2000, v253\n.endif\nglobal_load_dwordx4 %[x7], v254, %[gx] offset:((%c[par]+7)&1)*2048 sc1\nglobal_load_dwordx4 %[y7], v253, %[gx] offset:((%c[par]+7)&1)*2048 sc1\n"
    "s_waitcnt vmcnt(14)\n\tv_max3_u32 v255, %[a], %[b], %[c]\n\tv_max3_u32 v255, v255, %[d], %[e]\n\tv_max3_u32 v255, v255, %[f], %[g]\n\tv_max_u32 v255, v255, %[h]\n\tv_cmp_eq_u32 vcc, -1, v255\n"
    "s_cbranch_vccz .Lhd%c[id]\n\ts_sleep " STR(RSLP) "\n\ts_sub_u32 s95, s95, 1\n\ts_cmp_lg_u32 s95, 0\n\ts_cbranch_scc1 .Lhp%c[id]\n.Lhd%c[id]:\n"
    ".subsection 0"
    : [x0] "+v"(x0), [y0] "+v"(y0), [x1] "+v"(x1), [y1] "+v"(y1), [x2] "+v"(x2), [y2] "+v"(y2), [x3] "+v"(x3), [y3] "+v"(y3), [x4] "+v"(x4), [y4] "+v"(y4), [x5] "+v"(x5), [y5] "+v"(y5), [x6] "+v"(x6), [y6] "+v"(y6), [x7] "+v"(x7), [y7] "+v"(y7)
    : [m] "s"(m), [xv0] "v"(xv0), [xv1] "v"(xv1), [gx] "s"(gx), [a] "v"(x0[0]), [b] "v"(x0[1]), [c] "v"(x0[2]), [d] "v"(x0[3]),
      [e] "v"(y0[0]), [f] "v"(y0[1]), [g] "v"(y0[2]), [h] "v"(y0[3]), [id] "n"(ID), [par] "n"(PAR), [off] "n"(OFF)
    : "memory", "scc", "vcc", "s95", "v253", "v254", "v255");
}
template <int ID, int PAR> DI void hole16b(const char* gx) {
  asm volatile(".subsection 1\n"
    "s_branch .Lhb%c[id]\n\t.subsection 0"
    :
    : [gx] "s"(gx), [id] "n"(ID), [par] "n"(PAR)
    : "memory", "v253", "v254");
}
template <int ID, int PAR> DI void hole16b(const char* gx, u32x4& x0, u32x4& y0) {
  asm volatile(".subsection 1\n"
    ".if ((%c[par]+0)&1) == 0\nv_add_u32 v254, 0x2000, v254\nv_add_u32 v253, 0x2000, v253\n.endif\nglobal_load_dwordx4 %[x0], v254, %[gx] offset:((%c[par]+0)&1)*2048 sc1\nglobal_load_dwordx4 %[y0], v253, %[gx] offset:((%c[par]+0)&1)*2048 sc1\n"
    "s_branch .Lhb%c[id]\n\t.subsection 0"
    : [x0] "+v"(x0), [y0] "+v"(y0)
    : [gx] "s"(gx), [id] "n"(ID), [par] "n"(PAR)
    : "memory", "v253", "v254");
}
template <int ID, int PAR> DI void hole16b(const char* gx, u32x4& x0, u32x4& y0, u32x4& x1, u32x4& y1) {
  asm volatile(".subsection 1\n"
    ".if ((%c[par]+0)&1) == 0\nv_add_u32 v254, 0x2000, v254\nv_add_u32 v253, 0x2000, v253\n.endif\nglobal_load_dwordx4 %[x0], v254, %[gx] offset:((%c[par]+0)&1)*2048 sc1\nglobal_load_dwordx4 %[y0], v253, %[gx] offset:((%c[par]+0)&1)*2048 sc1\n"
    ".if ((%c[par]+1)&1) == 0\nv_add_u32 v254, 0x2000, v254\nv_add_u32 v253, 0x2000, v253\n.endif\nglobal_load_dwordx4 %[x1], v254, %[gx] offset:((%c[par]+1)&1)*2048 sc1\nglobal_load_dwordx4 %[y1], v253, %[gx] offset:((%c[par]+1)&1)*2048 sc1\n"
    "s_branch .Lhb%c[id]\n\t.subsection 0"
    : [x0] "+v"(x0), [y0] "+v"(y0), [x1] "+v"(x1), [y1] "+v"(y1)
    : [gx] "s"(gx), [id] "n"(ID), [par] "n"(PAR)
    : "memory", "v253", "v254");
}
template <int ID, int PAR> DI void hole16b(const char* gx, u32x4& x0, u32x4& y0, u32x4& x1, u32x4& y1, u32x4& x2, u32x4& y2) {
  asm volatile(".subsection 1\n"
    ".if ((%c[par]+0)&1) == 0\nv_add_u32 v254, 0x2000, v254\nv_add_u32 v253, 0x2000, v253\n.endif\nglobal_load_dwordx4 %[x0], v254, %[gx] offset:((%c[par]+0)&1)*2048 sc1\nglobal_load_dwordx4 %[y0], v253, %[gx] offset:((%c[par]+0)&1)*2048 sc1\n"
    ".if ((%c[par]+1)&1) == 0\nv_add_u32 v254, 0x2000, v254\nv_add_u32 v253, 0x2000, v253\n.endif\nglobal_load_dwordx4 %[x1], v254, %[gx] offset:((%c[par]+1)&1)*2048 sc1\nglobal_load_dwordx4 %[y1], v253, %[gx] offset:((%c[par]+1)&1)*2048 sc1\n"
    ".if ((%c[par]+2)&1) == 0\nv_add_u32 v254, 0x2000, v254\nv_add_u32 v253, 0x2000, v253\n.endif\nglobal_load_dwordx4 %[x2], v254, %[gx] offset:((%c[par]+2)&1)*2048 sc1\nglobal_load_dwordx4 %[y2], v253, %[gx] offset:((%c[par]+2)&1)*2048 sc1\n"
    "s_branch .Lhb%c[id]\n\t.subsection 0"
    : [x0] "+v"(x0), [y0] "+v"(y0), [x1] "+v"(x1), [y1] "+v"(y1), [x2] "+v"(x2), [y2] "+v"(y2)
    : [gx] "s"(gx), [id] "n"(ID), [par] "n"(PAR)
    : "memory", "v253", "v254");
}
template <int ID, int PAR> DI void hole16b(const char* gx, u32x4& x0, u32x4& y0, u32x4& x1, u32x4& y1, u32x4& x2, u32x4& y2, u32x4& x3, u32x4& y3) {
  asm volatile(".subsection 1\n"
    ".if ((%c[par]+0)&1) == 0\nv_add_u32 v254, 0x2000, v254\nv_add_u32 v253, 0x2000, v253\n.endif\nglobal_load_dwordx4 %[x0], v254, %[gx] offset:((%c[par]+0)&1)*2048 sc1\nglobal_load_dwordx4 %[y0], v253, %[gx] offset:((%c[par]+0)&1)*2048 sc1\n"
    ".if ((%c[par]+1)&1) == 0\nv_add_u32 v254, 0x2000, v254\nv_add_u32 v253, 0x2000, v253\n.endif\nglobal_load_dwordx4 %[x1], v254, %[gx] offset:((%c[par]+1)&1)*2048 sc1\nglobal_load_dwordx4 %[y1], v253, %[gx] offset:((%c[par]+1)&1)*2048 sc1\n"
    ".if ((%c[par]+2)&1) == 0\nv_add_u32 v254, 0x2000, v254\nv_add_u32 v253, 0x2000, v253\n.endif\nglobal_load_dwordx4 %[x2], v254, %[gx] offset:((%c[par]+2)&1)*2048 sc1\nglobal_load_dwordx4 %[y2], v253, %[gx] offset:((%c[par]+2)&1)*2048 sc1\n"
    ".if ((%c[par]+3)&1) == 0\nv_add_u32 v254, 0x2000, v254\nv_add_u32 v253, 0x2000, v253\n.endif\nglobal_load_dwordx4 %[x3], v254, %[gx] offset:((%c[par]+3)&1)*2048 sc1\nglobal_load_dwordx4 %[y3], v253, %[gx] offset:((%c[par]+3)&1)*2048 sc1\n"
    "s_branch .Lhb%c[id]\n\t.subsection 0"
    : [x0] "+v"(x0), [y0] "+v"(y0), [x1] "+v"(x1), [y1] "+v"(y1), [x2] "+v"(x2), [y2] "+v"(y2), [x3] "+v"(x3), [y3] "+v"(y3)
    : [gx] "s"(gx), [id] "n"(ID), [par] "n"(PAR)
    : "memory", "v253", "v254");
}
template <int ID, int PAR> DI void hole16b(const char* gx, u32x4& x0, u32x4& y0, u32x4& x1, u32x4& y1, u32x4& x2, u32x4& y2, u32x4& x3, u32x4& y3, u32x4& x4, u32x4& y4) {
  asm volatile(".subsection 1\n"
    ".if ((%c[par]+0)&1) == 0\nv_add_u32 v254, 0x2000, v254\nv_add_u32 v253, 0x2000, v253\n.endif\nglobal_load_dwordx4 %[x0], v254, %[gx] offset:((%c[par]+0)&1)*2048 sc1\nglobal_load_dwordx4 %[y0], v253, %[gx] offset:((%c[par]+0)&1)*2048 sc1\n"
    ".if ((%c[par]+1)&1) == 0\nv_add_u32 v254, 0x2000, v254\nv_add_u32 v253, 0x2000, v253\n.endif\nglobal_load_dwordx4 %[x1], v254, %[gx] offset:((%c[par]+1)&1)*2048 sc1\nglobal_load_dwordx4 %[y1], v253, %[gx] offset:((%c[par]+1)&1)*2048 sc1\n"
    ".if ((%c[par]+2)&1) == 0\nv_add_u32 v254, 0x2000, v254\nv_add_u32 v253, 0x2000, v253\n.endif\nglobal_load_dwordx4 %[x2], v254, %[gx] offset:((%c[par]+2)&1)*2048 sc1\nglobal_load_dwordx4 %[y2], v253, %[gx] offset:((%c[par]+2)&1)*2048 sc1\n"
    ".if ((%c[par]+3)&1) == 0\nv_add_u32 v254, 0x2000, v254\nv_add_u32 v253, 0x2000, v253\n.endif\nglobal_load_dwordx4 %[x3], v254, %[gx] offset:((%c[par]+3)&1)*2048 sc1\nglobal_load_dwordx4 %[y3], v253, %[gx] offset:((%c[par]+3)&1)*2048 sc1\n"
    ".if ((%c[par]+4)&1) == 0\nv_add_u32 v254, 0x2000, v254\nv_add_u32 v253, 0x2000, v253\n.endif\nglobal_load_dwordx4 %[x4], v254, %[gx] offset:((%c[par]+4)&1)*2048 sc1\nglobal_load_dwordx4 %[y4], v253, %[gx] offset:((%c[par]+4)&1)*2048 sc1\n"
    "s_branch .Lhb%c[id]\n\t.subsection 0"
    : [x0] "+v"(x0), [y0] "+v"(y0), [x1] "+v"(x1), [y1] "+v"(y1), [x2] "+v"(x2), [y2] "+v"(y2), [x3] "+v"(x3), [y3] "+v"(y3), [x4] "+v"(x4), [y4] "+v"(y4)
    : [gx] "s"(gx), [id] "n"(ID), [par] "n"(PAR)
    : "memory", "v253", "v254");
}
template <int ID, int PAR> DI void hole16b(const char* gx, u32x4& x0, u32x4& y0, u32x4& x1, u32x4& y1, u32x4& x2, u32x4& y2, u32x4& x3, u32x4& y3, u32x4& x4, u32x4& y4, u32x4& x5, u32x4& y5) {
  asm volatile(".subsection 1\n"
    ".if ((%c[par]+0)&1) == 0\nv_add_u32 v254, 0x2000, v254\nv_add_u32 v253, 0x2000, v253\n.endif\nglobal_load_dwordx4 %[x0], v254, %[gx] offset:((%c[par]+0)&1)*2048 sc1\nglobal_load_dwordx4 %[y0], v253, %[gx] offset:((%c[par]+0)&1)*2048 sc1\n"
    ".if ((%c[par]+1)&1) == 0\nv_add_u32 v254, 0x2000, v254\nv_add_u32 v253, 0x2000, v253\n.endif\nglobal_load_dwordx4 %[x1], v254, %[gx] offset:((%c[par]+1)&1)*2048 sc1\nglobal_load_dwordx4 %[y1], v253, %[gx] offset:((%c[par]+1)&1)*2048 sc1\n"
    ".if ((%c[par]+2)&1) == 0\nv_add_u32 v254, 0x2000, v254\nv_add_u32 v253, 0x2000, v253\n.endif\nglobal_load_dwordx4 %[x2], v254, %[gx] offset:((%c[par]+2)&1)*2048 sc1\nglobal_load_dwordx4 %[y2], v253, %[gx] offset:((%c[par]+2)&1)*2048 sc1\n"
    ".if ((%c[par]+3)&1) == 0\nv_add_u32 v254, 0x2000, v254\nv_add_u32 v253, 0x2000, v253\n.endif\nglobal_load_dwordx4 %[x3], v254, %[gx] offset:((%c[par]+3)&1)*2048 sc1\nglobal_load_dwordx4 %[y3], v253, %[gx] offset:((%c[par]+3)&1)*2048 sc1\n"
    ".if ((%c[par]+4)&1) == 0\nv_add_u32 v254, 0x2000, v254\nv_add_u32 v253, 0x2000, v253\n.endif\nglobal_load_dwordx4 %[x4], v254, %[gx] offset:((%c[par]+4)&1)*2048 sc1\nglobal_load_dwordx4 %[y4], v253, %[gx] offset:((%c[par]+4)&1)*2048 sc1\n"
    ".if ((%c[par]+5)&1) == 0\nv_add_u32 v254, 0x2000, v254\nv_add_u32 v253, 0x2000, v253\n.endif\nglobal_load_dwordx4 %[x5], v254, %[gx] offset:((%c[par]+5)&1)*2048 sc1\nglobal_load_dwordx4 %[y5], v253, %[gx] offset:((%c[par]+5)&1)*2048 sc1\n"
    "s_branch .Lhb%c[id]\n\t.subsection 0"
    : [x0] "+v"(x0), [y0] "+v"(y0), [x1] "+v"(x1), [y1] "+v"(y1), [x2] "+v"(x2), [y2] "+v"(y2), [x3] "+v"(x3), [y3] "+v"(y3), [x4] "+v"(x4), [y4] "+v"(y4), [x5] "+v"(x5), [y5] "+v"(y5)
    : [gx] "s"(gx), [id] "n"(ID), [par] "n"(PAR)
    : "memory", "v253", "v254");
}
template <int N> DI u64 test4(const u32x4& x) {
  u64 m; unsigned t;
  asm volatile("s_waitcnt vmcnt(%6)\n\tv_max3_u32 %1, %2, %3, %4\n\tv_max_u32 %1, %1, %5\n\tv_cmp_eq_u32_e64 %0, -1, %1"
               : "=s"(m), "=&v"(t) : "v"(x[0]), "v"(x[1]), "v"(x[2]), "v"(x[3]), "n"(N) : "memory");
  return m;
}
template <int N> DI unsigned test8a(const u32x4& x, const u32x4& y) {
  unsigned t;
  asm volatile("s_waitcnt vmcnt(%6)\n\tv_max3_u32 %0, %1, %2, %3\n\tv_max3_u32 %0, %0, %4, %5"
               : "=&v"(t) : "v"(x[0]), "v"(x[1]), "v"(x[2]), "v"(x[3]), "v"(y[0]), "n"(N) : "memory");
  return t;
}
DI u64 test8b(unsigned t, const u32x4& y) {
  u64 m;
  asm volatile("v_max3_u32 %1, %1, %2, %3\n\tv_max_u32 %1, %1, %4\n\tv_cmp_eq_u32_e64 %0, -1, %1" : "=s"(m), "+v"(t) : "v"(y[1]), "v"(y[2]), "v"(y[3]));
  return m;
}
template <int ID, int W, int J, int R, size_t... I> DI void fix32(u64 m, unsigned xv, const char* gx, u32x4 (&xr)[R], std::index_sequence<I...>) {
  hole32<ID + J, W, J * 1024 + 4096>(m, xv, gx, xr[(J + I) % R]...);
}
template <int R, int J> struct Pair { u32x4 (&a)[R]; u32x4 (&b)[R]; };
template <int ID, int J, int R> DI void fix16a(u64 m, unsigned v0, unsigned v1, const char* gx, u32x4 (&x)[R], u32x4 (&y)[R], std::index_sequence<0>) {
  hole16a<ID + J, J & 1, (J >> 1) * 8192>(m, v0, v1, gx, x[J % R], y[J % R]);
}
#define FIXA(...) hole16a<ID + J, J & 1, (J >> 1) * 8192>(m, v0, v1, gx, __VA_ARGS__)
#define XY(i) x[(J + i) % R], y[(J + i) % R]
template <int ID, int J, int W, int R> DI void fix16w(u64 m, unsigned v0, unsigned v1, const char* gx, u32x4 (&x)[R], u32x4 (&y)[R]) {
  static_assert(W >= 1 && W <= 7, "window");
  if constexpr (W == 1) FIXA(XY(0));
  if constexpr (W == 2) FIXA(XY(0), XY(1));
  if constexpr (W == 3) FIXA(XY(0), XY(1), XY(2));
  if constexpr (W == 4) FIXA(XY(0), XY(1), XY(2), XY(3));
  if constexpr (W == 5) FIXA(XY(0), XY(1), XY(2), XY(3), XY(4));
  if constexpr (W == 6) FIXA(XY(0), XY(1), XY(2), XY(3), XY(4), XY(5));
  if constexpr (W == 7) FIXA(XY(0), XY(1), XY(2), XY(3), XY(4), XY(5), XY(6));
}
#define XZ(i) , x[(J + W + i) % R], y[(J + W + i) % R]
template <int ID, int J, int W, int N, int R> DI void fix16r(const char* gx, u32x4 (&x)[R], u32x4 (&y)[R]) {
  constexpr int P = (J + W) & 1;
  static_assert(N <= 6, "rest");
  if constexpr (N == 0) hole16b<ID + J, P>(gx);
  if constexpr (N == 1) hole16b<ID + J, P>(gx XZ(0));
  if constexpr (N == 2) hole16b<ID + J, P>(gx XZ(0) XZ(1));
  if constexpr (N == 3) hole16b<ID + J, P>(gx XZ(0) XZ(1) XZ(2));
  if constexpr (N == 4) hole16b<ID + J, P>(gx XZ(0) XZ(1) XZ(2) XZ(3));
  if constexpr (N == 5) hole16b<ID + J, P>(gx XZ(0) XZ(1) XZ(2) XZ(3) XZ(4));
  if constexpr (N == 6) hole16b<ID + J, P>(gx XZ(0) XZ(1) XZ(2) XZ(3) XZ(4) XZ(5));
}
template <int ID, int NA, int R, int RB, bool BEND, int NT, int WIN, class FW, class FT>
DI void gemm32x(f32x16 (&acc)[NA], const char* gx, unsigned xv, bool rbar, FW&& waddr, FT&& tab FARG) {
  constexpr int KS = 40;
  bf16x8 w[3][NA];
  u32x4 xr[R];
  u64 miss;
  const char* g = gx + 4096;
  opaque_s(g);
  auto ldx = [&](auto k) {
    gld16<((k & 7) - 4) * 1024>(xr[k % R], xv, g);
    if constexpr ((k & 7) == 7) { g += 8192; opaque_s(g); }
  };
  auto fix = [&](auto j, auto je) {
    constexpr int n = je - j;
    fix32<ID, (WIN < n ? WIN : n), j, R>(miss, xv, gx, xr, std::make_index_sequence<n>{});
  };
  static_for<0, NA>([&](auto a) { keep(acc[a]); });
  bar();
  sbar();
  TSF(1);
  static_for<0, R>([&](auto k) { ldx(k); });
  static_for<0, NA>([&](auto r) { w[0][r] = lds_rd128(waddr(I0{}) + r * 512); });
  static_for<0, NA>([&](auto r) { w[1][r] = lds_rd128(waddr(I1{}) + r * 512); });
  sbar();
  miss = test4<R - 1>(xr[0]);
  fix(I0{}, IC<R>{});
  sbar();
  static_for<0, KS>([&](auto k) {
    if constexpr (k == RB || (RB > 0 && PST && (k == 6 || k == 14))) { if (rbar) bar(); sbar(); }
    if constexpr ((k & 3) == 0) TSF(2 + k / 4);
    constexpr int T0 = (NT + 1) / 2;
    constexpr int ND = k + 2 < KS ? NA : (k + 2 == KS ? T0 : NT - T0);
    inter<NA, ND, (k + 2 < KS ? 1 : 2)>([&](auto m) {
      mfma32u(acc[m], w[k % 3][m], xr[k % R]);
      if constexpr (m == NA - 2 && k + 1 < KS) { sbar(); miss = test4<(k + R < KS ? k + R : KS) - (k + 1) - 1>(xr[(k + 1) % R]); }
    }, [&](auto r) {
      if constexpr (k + 2 < KS) w[(k + 2) % 3][r] = lds_rd128(waddr(IC<k + 2>{}) + r * 512);
      else tab(IC<(k + 2 == KS ? 0 : T0) + r>{});
    });
    if constexpr (k + R < KS) { ldx(IC<k + R>{}); sbar(); }
    if constexpr (k + 1 < KS) { fix(IC<k + 1>{}, IC<(k + R + 1 < KS ? k + R + 1 : KS)>{}); sbar(); }
  });
  if constexpr (BEND) wait_lgkm_bar();
  static_for<0, NA>([&](auto a) { drain1<(a == 0)>(acc[a]); });
  sbar();
}
template <int ID, int NK, int R, bool RING, int WIN, class FW>
DI void gemm16x(f32x4 (&acc0)[5], f32x4 (&acc1)[5], const char* gx, unsigned xv0, unsigned xv1, FW&& waddr, unsigned exw FARG) {
  constexpr int KS = 2 * NK;
  bf16x8 w[3][5];
  u32x4 x0[R], x1[R];
  u64 miss;
  unsigned tmax;
  const char* g = gx;
  opaque_s(g);
  auto ldx = [&](auto k) {
    gld16<(k & 1) * 2048>(x0[k % R], xv0, g);
    gld16<(k & 1) * 2048>(x1[k % R], xv1, g);
    if constexpr (k & 1) { g += 8192; opaque_s(g); }
  };
  auto fix = [&](auto j, auto je) {
    constexpr int n = je - j, wn = WIN < n ? WIN : n;
    fix16w<ID, j, wn, R>(miss, xv0, xv1, gx, x0, x1);
    fix16r<ID, j, wn, n - wn, R>(gx, x0, x1);
  };
  static_for<0, 5>([&](auto m) { keep(acc0[m]); keep(acc1[m]); });
  bar();
  sbar();
  TSF(1);
  constexpr int R0 = R < KS ? R : KS;
  static_for<0, R0>([&](auto k) { ldx(k); });
  static_for<0, 5>([&](auto r) { w[0][r] = lds_rd128(waddr(I0{}) + r * 256); });
  static_for<0, 5>([&](auto r) { w[1][r] = lds_rd128(waddr(I1{}) + r * 256); });
  sbar();
  tmax = test8a<2 * (R0 - 1)>(x0[0], x1[0]);
  miss = test8b(tmax, x1[0]);
  fix(I0{}, IC<R0>{});
  sbar();
  static_for<0, KS - 2>([&](auto k) {
    if constexpr (RING && k >= 4 && (k & 3) == 0) { bar(); sbar(); }
    if constexpr (!RING && WB4 && k == 6) { bar(); sbar(); }
    if constexpr ((k & 1) == 0) TSF(2 + k / 2);
    inter<10, 5, 1>([&](auto m) {
      if constexpr (m & 1) mfma16u(acc1[m / 2], w[k % 3][m / 2], x1[k % R]); else mfma16u(acc0[m / 2], w[k % 3][m / 2], x0[k % R]);
      if constexpr (m == 6) { sbar(); tmax = test8a<2 * ((k + R < KS ? k + R : KS) - (k + 1) - 1)>(x0[(k + 1) % R], x1[(k + 1) % R]); }
      if constexpr (m == 7) { sbar(); miss = test8b(tmax, x1[(k + 1) % R]); }
    }, [&](auto r) { w[(k + 2) % 3][r] = lds_rd128(waddr(IC<k + 2>{}) + r * 256); });
    if constexpr (k + R < KS) { ldx(IC<k + R>{}); sbar(); }
    fix(IC<k + 1>{}, IC<(k + R + 1 < KS ? k + R + 1 : KS)>{});
    sbar();
  });
  {
    constexpr int ka = KS - 2, kb = KS - 1;
    static_for<0, 3>([&](auto m) { mfma16u(acc1[m], w[ka % 3][m], x1[ka % R]); });
    sbar();
    tmax = test8a<0>(x0[kb % R], x1[kb % R]);
    mfma16u(acc1[3], w[ka % 3][3], x1[ka % R]);
    miss = test8b(tmax, x1[kb % R]);
    mfma16u(acc1[4], w[ka % 3][4], x1[ka % R]);
    fix(IC<kb>{}, IC<KS>{});
    sbar();
    static_for<0, 5>([&](auto m) { mfma16u(acc1[m], w[kb % 3][m], x1[kb % R]); });
    static_for<0, 5>([&](auto m) { mfma16u(acc0[m], w[ka % 3][m], x0[ka % R]); });
    sbar();
    static_for<0, 5>([&](auto m) { mfma16u(acc0[m], w[kb % 3][m], x0[kb % R]); sbar(); *(LAS f32x4*)(exw + m * 1024) = acc1[m]; sbar(); });
    wait_lgkm_bar();
    static_for<0, 5>([&](auto m) { drain1<(m == 0)>(acc0[m]); });
    sbar();
  }
}

DI void stats_token(const char* stg, int l, unsigned ms_addr) {
  const unsigned so = l * 8;
  u32x2 sv[8];
#pragma unroll 1
  for (int it = 0; it < SPIN_MAX; it++) {
    static_for<0, 8>([&](auto c) { gld8<(c & 3) * 512>(sv[c], so, stg + (c >> 2) * 2048); });
    wait_vm<0>();
    static_for<0, 8>([&](auto c) { keep(sv[c]); });
    unsigned mx = 0;
    static_for<0, 8>([&](auto c) { mx = umax(umax(mx, sv[c][0]), sv[c][1]); });
    if (__builtin_amdgcn_ballot_w64(mx == HOLE) == 0) break;
    __builtin_amdgcn_s_sleep(RSLP);
  }
  f32x2 p[8];
#pragma unroll
  for (int c = 0; c < 8; c++) p[c] = (f32x2){__uint_as_float(sv[c][0]), __uint_as_float(sv[c][1])};
  const f32x2 t = ((p[0] + p[1]) + (p[2] + p[3])) + ((p[4] + p[5]) + (p[6] + p[7]));
  const float m = t[0] * 0.125f;
  float e[8];
#pragma unroll
  for (int c = 0; c < 8; c++) { e[c] = p[c][0] - m; e[c] *= e[c]; }
  const float e2 = ((e[0] + e[1]) + (e[2] + e[3])) + ((e[4] + e[5]) + (e[6] + e[7]));
  const float rstd = __builtin_amdgcn_rsqf((t[1] + 80.f * e2) * (1.0f / 640.f) + LN_EPS);
  *(LAS u32x2*)ms_addr = (u32x2){fillw(__float_as_uint(m)), fillw(__float_as_uint(rstd))};
}
DI void ms_ready(f32x2& ms, unsigned a) {
#pragma unroll 1
  for (int it = 0; it < SPIN_MAX; it++) {
    if (!any_hole(umax(__float_as_uint(ms[0]), __float_as_uint(ms[1])))) break;
    ms = *(volatile const LAS f32x2*)a;
  }
}
template <int J0, int J1, int SLP, int N> DI void park_load(u32x4 (&d)[N], const char* g, int l) {
  const unsigned v0 = l * 16;
  const char* gp = g + 4096 + (J0 >> 3) * 8192;
  opaque_s(gp);
  static_for<J0, J1>([&](auto j) {
    gld16<((j & 7) - 4) * 1024>(d[j], v0, gp);
    if constexpr ((j & 7) == 7) { gp += 8192; opaque_s(gp); }
    if constexpr (SLP > 0) __builtin_amdgcn_s_sleep(SLP);
  });
}
template <int N> DI void park_keep(u32x4 (&d)[N]) { static_for<0, N>([&](auto j) { keep(d[j]); }); }
template <int N> DI void park_put(u32x4 (&d)[N], unsigned lds) {
  static_for<0, N>([&](auto j) { *(LAS u32x4*)(lds + j * 1024) = d[j]; });
}
template <int N, bool HALF, int SLP> DI void fill_holes(rsrc_t rs, unsigned so, int l) {
  const u32x4 hole = {HOLE, HOLE, HOLE, HOLE};
  static_for<0, N>([&](auto j) { st16(rs, l * 16 + j * 1024, so, hole); if constexpr (SLP > 0) __builtin_amdgcn_s_sleep(SLP); });
  if constexpr (HALF) { if (l < 32) st16(rs, l * 16 + N * 1024, so, hole); }
}
template <int WST> struct WLoad {
  unsigned v0, v1, v2, lds;
  const char* g;
  DI WLoad(const char* g_, unsigned lds_, int l) : lds(lds_), g(g_) {
    v0 = l * 16; keep(v0); v1 = v0 + 4096; v2 = v0 + 8192;
    opaque_s(g); opaque_s(lds);
  }
  template <int DST> DI void stage(int l) {
    constexpr int HB = WST / 2;
    dma4<DST>(v0, g, lds);
    if constexpr (HB == 6144) dma2<DST + 4096>(v1, g, lds);
    if constexpr (HB == 7680) { dma3<DST + 4096>(v1, g, lds); if (l < 32) dma1<DST + 4096, 3072>(v1, g, lds); }
    if constexpr (HB == 10240) { dma4<DST + 4096>(v1, g, lds); dma2<DST + 8192>(v2, g, lds); }
    g += WST; opaque_s(g);
  }
};
struct DstQ { static constexpr int at(int s) { return s < 6 ? 65536 + s * 15360 : (s - 6) * 15360; } };
struct DstO { static constexpr int at(int s) { return s < 4 ? 73728 + s * 20480 : 0; } };
struct Dst1 { static constexpr int at(int s) { return s * 12288; } };
struct Dst2 { static constexpr int at(int s) { return (s % 7) * 20480; } };
template <int WST, class FD, int S0, int S1> DI void wissue(const char* g0, int half, int l) {
  WLoad<WST> W(g0 + (size_t)S0 * WST + half * (WST / 2), (unsigned)half * (WST / 2), l);
  static_for<S0, S1>([&](auto s) { W.template stage<FD::at(s)>(l); });
}

#define MAP_VARS [[maybe_unused]] const int rbA = (xcd >> CG) * (2 << CG) + (jj >> (4 - CG)), ctA = (xcd & ((1 << CG) - 1)) * (16 >> CG) + (jj & ((16 >> CG) - 1)), \
  rbB = 2 * ((xcd >> CGB) * (2 << CGB) + (jj >> (4 - CGB))) + ((jj >> (3 - CGB)) & 1), ctB = (xcd & ((1 << CGB) - 1)) * (8 >> CGB) + (jj & ((8 >> CGB) - 1)), \
  mB = (rbB & 1) * 8 + ctB;
#define LANE_VARS int l = l0, bid = bid0, w = w0; keep(l); opaque_s(bid); opaque_s(w); \
  [[maybe_unused]] const int g = l >> 4, i = l & 15, h = l >> 5, a31 = l & 31, xcd = bid & 7, jj = bid >> 3; \
  MAP_VARS
DI unsigned flag_off(int edge, int grp, int member) { return WS_FE + (unsigned)edge * FE_STR + (unsigned)(grp * 16 + member) * FSTR; }
constexpr unsigned L_EX = 32768, L_EX2 = 102400;

__global__ __launch_bounds__(512, 2) void k_all(const u16* __restrict__ x, const u16* __restrict__ wqkv, const u16* __restrict__ wo,
                                                 const u16* __restrict__ w1, const u16* __restrict__ b1, const u16* __restrict__ w2,
                                                 const u16* __restrict__ b2, u16* __restrict__ out, char* __restrict__ ws) {
  constexpr unsigned lb = 0;
  asm volatile("" ::"s"(x), "s"(wqkv), "s"(wo), "s"(w1), "s"(b1), "s"(w2), "s"(b2), "s"(out), "s"(ws));
  const int tid = threadIdx.x, l = tid & 63, l0 = l, w = __builtin_amdgcn_readfirstlane(tid >> 6), w0 = w;
  const int bid = blockIdx.x, bid0 = bid, xcd = bid & 7, jj = bid >> 3;
  MAP_VARS
  const int g = l >> 4, i = l & 15, h = l >> 5, a31 = l & 31;
  const rsrc_t rs = __builtin_amdgcn_make_buffer_rsrc(ws, 0, 0x7fffffff, 0x00020000);
  TS(0);
#define CPF 0
  const int c8 = xcd, sl = jj;
  u32x2 ev;
  gld8<0>(ev, 0u, ws);
  u32x2 res[5];
  unsigned biasv = 0;
  auto tr_unit = [&](unsigned ad, unsigned ad2) { return cat(lds_rdtr(ad), lds_rdtr(ad2)); };
  auto sum8 = [&](bf16x8 v) {
    const u32x4 uv = __builtin_bit_cast(u32x4, v);
    return ((bflo(uv[0]) + bfhi(uv[0])) + (bflo(uv[1]) + bfhi(uv[1]))) + ((bflo(uv[2]) + bfhi(uv[2])) + (bflo(uv[3]) + bfhi(uv[3])));
  };
  const int tr_r = i >> 2, tr_q = i & 3;
  auto put_stats = [&](const u32x2(&r)[5], int stbuf, int l, int w, int rbB, int ctB) {
    const int g = l >> 4, i = l & 15;
    float v[5][4], sum = 0.f;
#pragma unroll
    for (int m = 0; m < 5; m++) {
      v[m][0] = bflo(r[m][0]); v[m][1] = bfhi(r[m][0]); v[m][2] = bflo(r[m][1]); v[m][3] = bfhi(r[m][1]);
      sum += (v[m][0] + v[m][1]) + (v[m][2] + v[m][3]);
    }
    sum = swap32_add(swap16_add(sum));
    const float mb = sum * (1.0f / 80.f);
    float m2 = 0.f;
#pragma unroll
    for (int m = 0; m < 5; m++)
#pragma unroll
      for (int e = 0; e < 4; e++) { const float d = v[m][e] - mb; m2 += d * d; }
    m2 = swap32_add(swap16_add(m2));
    if (g == 0) st8(rs, ((w & 1) * 32 + (w >> 1) * 16 + i) * 8, WS_ST + stbuf * 131072 + (rbB * 8 + ctB) * 512, (u32x2){fillw(__float_as_uint(mb)), fillw(__float_as_uint(m2))});
  };
  auto res_chunks = [&](u32x4(&c)[3]) {
#pragma unroll
    for (int p = 0; p < 3; p++) {
      unsigned a0 = res[2 * p][0], a1 = res[2 * p][1], b0 = res[p < 2 ? 2 * p + 1 : 4][0], b1_ = res[p < 2 ? 2 * p + 1 : 4][1];
      sw16(a0, b0); sw16(a1, b1_);
      c[p] = (u32x4){a0, a1, b0, b1_};
    }
  };
  auto store_res = [&](unsigned xdst, int l, int w, int rbB, int ctB) {
    const int g = l >> 4, i = l & 15;
    const int tok = (w & 1) * 32 + (w >> 1) * 16 + i, ch = (g & 1) * 2 + (g >> 1);
    u32x4 c[3];
    res_chunks(c);
    const unsigned vo = (tok & 31) * 16, so = xdst + ((rbB * 2 + (w & 1)) * 80 + ctB * 10) * 512;
    st16(rs, vo + ch * 512, so, c[0]);
    st16(rs, vo + (4 + ch) * 512, so, c[1]);
    if ((g & 1) == 0) st16(rs, vo + (8 + (g >> 1)) * 512, so, c[2]);
  };
  auto w2_tiles = [&](int t0, int dt) {
    for (int t = t0; t < 8; t += dt) {
      int uq = 64 * t + (l & 48) + 4 * tr_q;
      uq = uq < 476 ? uq : 476;
      const int c = uq / 80, n = uq - 80 * c;
      const unsigned ad = lb + L_R2 + (8 * c + tr_r) * 160 + n * 2;
      const bf16x8 v = tr_unit(ad, ad + 4 * 160);
      const int u = 64 * t + l;
      const int cu = u / 80, r = 6 * (sl & 1) + cu;
      if (u < 480) st16(rs, (64 * (r >> 2) + (r & 3)) * 1280 + (u - 80 * cu) * 16, WS_W2 + c8 * 245760 + (sl >> 1) * 5120, __builtin_bit_cast(u32x4, v));
    }
  };
  auto wo_tiles = [&](int t0, int dt) {
    for (int t = t0; t < 7; t += dt) {
      int uq = 64 * t + (l & 48) + 4 * tr_q;
      uq = uq < 396 ? uq : 396;
      const int c = uq / 80, n = uq - 80 * c;
      const int r0 = (c < 4 ? (c >> 1) * 20 + (c & 1) * 8 : 16) + tr_r, dr = c < 4 ? 4 : 20;
      const unsigned ad = lb + L_RO + r0 * 160 + n * 2;
      const bf16x8 v = tr_unit(ad, ad + dr * 160);
      const int u = 64 * t + l;
      if (u < 400) st16(rs, u * 16, WS_WO + c8 * 102400 + sl * 6400, __builtin_bit_cast(u32x4, v));
    }
  };
  u64 E;
  auto epoch = [&]() { keep(ev); E = (((u64)(unsigned)__builtin_amdgcn_readfirstlane(ev[1]) << 32) | (unsigned)__builtin_amdgcn_readfirstlane(ev[0])) + 1; };
  if (w < 4) {
    const unsigned xo = ((rbB * 64 + (w & 1) * 32 + (w >> 1) * 16 + i) * 640 + ctB * 80 + g * 4) * 2;
    static_for<0, 5>([&](auto m) { gld8<m * 32>(res[m], xo, x); });
    wait_vm<0>();
    epoch();
    static_for<0, 5>([&](auto m) { keep(res[m]); res[m] = (u32x2){fillw(res[m][0]), fillw(res[m][1])}; });
    store_res(WS_X2, l, w, rbB, ctB);
    put_stats(res, 0, l, w, rbB, ctB);
    publish_wave(ws, WS_PX4 + (((rbB >> 1) * 16 + mB) * 4 + w) * 8, E, l);
    TSP(1);
    wait_lgkm_bar();
    TSP(2);
#pragma unroll
    for (int t = 0; t < 2; t++) {
      int uq = 64 * t + (l & 48) + 4 * tr_q;
      uq = uq < 116 ? uq : 116;
      const int c = uq / 24, n = uq - 24 * c;
      const unsigned ad = lb + L_R1 + (8 * c + tr_r) * 192 + (24 * w + n) * 2;
      const bf16x8 v = tr_unit(ad, ad + 4 * 192);
      const int u = 64 * t + l;
      if (u < 120) {
        const int cu = u / 24, nu = u - 24 * cu;
        st16(rs, cu * 1536 + nu * 16, WS_W1 + ctA * 122880 + rbA * 7680 + w * 384, __builtin_bit_cast(u32x4, v));
        *(LAS float*)(lb + L_P1 + w * 512 + u * 4) = sum8(v);
      }
    }
    wait_lgkm();
    if (l < 24) {
      float s = 0.f;
#pragma unroll
      for (int c = 0; c < 5; c++) s += *(const LAS float*)(lb + L_P1 + w * 512 + (c * 24 + l) * 4);
      st4(rs, l * 4, WS_CS1 + (ctA * 16 + rbA) * 512 + w * 96, __float_as_uint(s));
    }
    w2_tiles(4 + w, 8);
    fill_holes<2, true, 0>(rs, WS_O + ((4 * rbA + w) * 80 + 5 * ctA) * 512, l);
    wait_vm<0>();
    if (arrive<8>(lb + L_CNT + 4, l)) st_flag(ws, WS_P2 + bid * 8, E);
  } else {
    const int p = w - 4;
#pragma unroll
    for (int m = 0; m < 5; m++) res[m] = (u32x2){0u, 0u};
    unsigned qo[2];
#pragma unroll
    for (int j = 0; j < 2; j++) {
      const int o = j * 1024 + (j == 1 && l >= 56 ? 55 : l) * 16, row = o / 240, wi = o - row * 240, seg = wi / 80;
      qo[j] = row * 3840 + seg * 1280 + (wi - seg * 80) - j * 1024;
    }
    const char* gq = (const char*)wqkv + (size_t)(rbA * 40 + p * 8) * 3840 + ctA * 80;
    dmab(qo[0], qo[1], gq, lb + L_RQ + p * 1920);
    if (p == 0) dmab(qo[0], qo[1], gq + 32 * 3840, lb + L_RQ + 4 * 1920);
    gld4<0>(biasv, (w == 4 ? ctA * 96 + (l < 48 ? l : 47) * 2 : ctB * 80 + (l < 40 ? l : 39) * 2) * 2, w == 4 ? b1 : b2);
    unsigned ta[2];
#pragma unroll
    for (int t = 0; t < 2; t++) {
      int n = 64 * t + (l & 48) + 4 * tr_q;
      n = n < 116 ? n : 116;
      const int seg = n / 40, qd = (n - 40 * seg) >> 2;
      const int sq = qd < 4 ? qd : (qd < 8 ? qd + 1 : (qd == 8 ? 4 : 9));
      ta[t] = lb + L_RQ + tr_r * 240 + seg * 80 + sq * 8;
    }
    auto retile = [&](int blk) {
      const u32x4 v0 = __builtin_bit_cast(u32x4, tr_unit(ta[0] + blk * 1920, ta[0] + blk * 1920 + 960));
      const u32x4 v1 = __builtin_bit_cast(u32x4, tr_unit(ta[1] + blk * 1920, ta[1] + blk * 1920 + 960));
      const unsigned so = WS_WQ + ctA * 153600 + (5 * rbA + blk) * 1920;
      st16(rs, l * 16, so, v0);
      if (l < 56) st16(rs, l * 16 + 1024, so, v1);
      *(LAS float*)(lb + L_PQ + (blk * 128 + l) * 4) = dot8(v0, 0.f);
      *(LAS float*)(lb + L_PQ + (blk * 128 + 64 + l) * 4) = dot8(v1, 0.f);
    };
    keep(ta[0]); keep(ta[1]);
    wait_vm<1>();
    retile(p);
    if (p == 0) retile(4);
    for (int pc = p; pc < 8; pc += 4) {
      const int o = pc * 1024 + l * 16;
      const int r1 = o / 192, r2 = o / 160;
      if (o < 7680) {
        dma16((rbA * 40 + r1) * 3072 + ctA * 192 + (o - r1 * 192), w1, lb + L_R1 + pc * 1024);
        dma16((sl * 48 + r2) * 1280 + c8 * 160 + (o - r2 * 160), w2, lb + L_R2 + pc * 1024);
      }
      if (sl < 16 && o < 6400) dma16((sl * 40 + r2) * 1280 + c8 * 160 + (o - r2 * 160), wo, lb + L_RO + pc * 1024);
    }
    if (w == 6) *(LAS u32x2*)(lb + L_ONE + l * 8) = (u32x2){0x00003f80u, 0u};
    if (w == 7 && l < 3) *(LAS unsigned*)(lb + L_CNT + l * 4) = 0u;
    if (sl < 16) { if (p == 3) wait_vm<SLW + SLW / 2 - (SLW ? 1 : 0)>(); else wait_vm<SLW + SLW / 2>(); } else wait_vm<SLW>();
    keep(biasv);
    epoch();
    if (l == 0) st_flag(ws, WS_PW4 + ((ctA * 16 + rbA) * 4 + p) * 8, E);
    if (w == 4 && l < 48) *(LAS f32x2*)(lb + L_B1 + l * 8) = (f32x2){bflo(biasv), bfhi(biasv)};
    if (w == 5 && l < 40) *(LAS f32x2*)(lb + L_B2 + l * 8) = (f32x2){bflo(biasv), bfhi(biasv)};
    wait_vm<1>();
    TSP(1);
    wait_lgkm_bar();
    TSP(2);
    if (w < 6) {
      const int f = (w - 4) * 64 + l;
      if (f < 120) {
        float s = 0.f;
#pragma unroll
        for (int c = 0; c < 5; c++) s += *(const LAS float*)(lb + L_PQ + (c * 128 + f) * 4);
        st4(rs, f * 4, WS_CSQ + (ctA * 16 + rbA) * 512, __float_as_uint(s));
      }
      if (sl < 16) wo_tiles(w - 4, 2);
      poll(ws, WS_PX4 + (rbA * 16 + p * 8) * 32 + (l & 31) * 8, l < 32, E);
      wait_vm<0>();
      if (arrive<2>(lb + L_CNT + 8, l)) st_flag(ws, WS_P1C + (ctA * 16 + rbA) * 8, E);
      if (arrive<8>(lb + L_CNT + 4, l)) st_flag(ws, WS_P2 + bid * 8, E);
    } else {
      for (int t = w - 6; t < 4; t += 2) w2_tiles(t, 8);
      poll(ws, WS_PW4 + ctA * 512 + l * 8, true, E);
      wait_vm<0>();
      if (arrive<8>(lb + L_CNT + 4, l)) st_flag(ws, WS_P2 + bid * 8, E);
      wissue<15360, DstQ, 0, 6>(ws + WS_WQ + ctA * 153600, w - 6, l);
    }
  }
  TSP(3);
  auto attention = [&](auto helper_, const int layer, auto&& hk) __attribute__((always_inline)) {
      constexpr bool HELPER = helper_;
      const int eb = 5 * layer;
      [[maybe_unused]] const int tb = 4 + layer * 24;
      TSB(2);
      LANE_VARS
      const int hd = w & 1, qt = HELPER ? (w < 6 ? 0 : 1) : (w < 2 ? 3 : 2);
      const int gmin = rbA == 0 ? 8 : (rbA == 1 ? 4 : 0);
      if (HELPER && w < 6) *(LAS u32x2*)(lb + L_MS + ((w - 4) * 64 + l) * 8) = (u32x2){HOLE, HOLE};
      if (HELPER && w < 6 && gmin < 8) {
        const int rr = rbA - 1 - ((l >> 2) & 1);
        poll(ws, flag_off(eb + 0, ctA, rr & 15) + (l & 3) * 8, l < 8 && rr >= 0, E);
        const char* src = ws + WS_KV + (size_t)((layer * 16 + ctA) * 2 + (w - 4)) * 163840 + (ptrdiff_t)(4 * rbA - 8) * 2560;
        const unsigned dst = lb + (w == 4 ? L_K : L_V);
        for (int p = 19; p >= (gmin == 4 ? 10 : 0); p--) dma16(l * 16 + p * 1024, src, dst + p * 1024);
      }
      if (!HELPER) publish_wave(ws, flag_off(eb + 0, ctA, rbA) + w * 8, E, l);
      TSB(3);
      const unsigned qa = lb + L_Q + qt * 2560 + a31 * 16;
      const bf16x8 q0f = lds_rd128(qa + (2 * hd + h) * 512);
      const s16x4 qt4 = lds_rd64(qa + 2048 + hd * 8);
      u32x4 q1u;
      { const u32x2 t = __builtin_bit_cast(u32x2, qt4); q1u = (u32x4){h ? 0u : t[0], h ? 0u : t[1], 0u, 0u}; }
      const unsigned ka0 = lb + L_K + (2 * hd + h) * 512 + a31 * 16, ka1 = lb + L_K + 2048 + a31 * 16 + hd * 8;
      const bool cst = (g & 1) && (i & 3) != 0;
      const unsigned vstr = cst ? 0u : 2560u;
      unsigned vaA, vaB;
      {
        const int k = 4 * h + (i >> 2);
        if (g & 1) {
          vaA = lb + L_V + 2048 + ((k ^ 8) << 4) + hd * 8; vaB = lb + L_V + 2048 + (((k + 8) ^ 8) << 4) + hd * 8;
          if (cst) { vaA = lb + L_ONE + (h ? 128 : 192); vaB = lb + L_ONE + (h ? 0 : 64); }
        } else {
          const int odd = (i >> 1) & 1;
          vaA = lb + L_V + (2 * hd + odd) * 512 + ((k ^ (odd * 4)) << 4) + (i & 1) * 8; vaB = vaA + 128;
        }
      }
      auto qk = [&](int G) {
        const bf16x8 k0 = lds_rd128(ka0 + G * 2560);
        const u32x2 t = __builtin_bit_cast(u32x2, lds_rd64(ka1 + G * 2560));
        const u32x4 k1 = {t[0], t[1], 0x3f803f80u, 0x00003f80u};
        f32x16 s;
#pragma unroll
        for (int r = 0; r < 16; r++) s[r] = 0.f;
        s = __builtin_amdgcn_mfma_f32_32x32x16_bf16(k0, q0f, s, 0, 0, 0);
        s = __builtin_amdgcn_mfma_f32_32x32x16_bf16(__builtin_bit_cast(bf16x8, k1), __builtin_bit_cast(bf16x8, q1u), s, 0, 0, 0);
        return s;
      };
      const float NEG = -__builtin_inff();
      auto maskit = [&](f32x16& s, int mode) {
        if (mode == 0) return;
        const int d = a31 - 4 * h;
#pragma unroll
        for (int r = 0; r < 16; r++) { const int b = (r & 3) + 8 * (r >> 2); const bool le = b <= d; s[r] = (le == (mode == 1)) ? s[r] : NEG; }
      };
      f32x16 o;
      auto pv = [&](const f32x16& s, int G) {
        unsigned pk[8];
#pragma unroll
        for (int r = 0; r < 8; r++) pk[r] = cvtpk(__builtin_amdgcn_exp2f(s[2 * r]), __builtin_amdgcn_exp2f(s[2 * r + 1]));
        const unsigned pA = vaA + G * vstr, pB = vaB + G * vstr;
        const bf16x8 v0 = cat(lds_rdtr(pA), lds_rdtr(pB)), v1 = cat(lds_rdtr(pA + 256), lds_rdtr(pB + 256));
        const u32x4 p0 = {pk[0], pk[1], pk[2], pk[3]}, p1 = {pk[4], pk[5], pk[6], pk[7]};
        o = __builtin_amdgcn_mfma_f32_32x32x16_bf16(v0, __builtin_bit_cast(bf16x8, p0), o, 0, 0, 0);
        o = __builtin_amdgcn_mfma_f32_32x32x16_bf16(v1, __builtin_bit_cast(bf16x8, p1), o, 0, 0, 0);
      };
      auto rowmax = [&](const f32x16& s) {
        float m = s[0];
#pragma unroll
        for (int r = 1; r < 16; r++) m = fmaxf(m, s[r]);
        return m;
      };
      auto setm = [&](float m) {
        const float nm = -m;
        const unsigned hi = __float_as_uint(nm) & 0xffff0000u;
        const float r1 = nm - __uint_as_float(hi);
        const unsigned mid = __float_as_uint(r1) & 0xffff0000u;
        const float r2 = r1 - __uint_as_float(mid);
        const unsigned lo = __float_as_uint(r2) & 0xffff0000u;
        q1u[2] = h ? 0u : ((hi >> 16) | mid);
        q1u[3] = h ? 0u : (lo >> 16);
      };
      {
        f32x16 s = qk(qt + 8);
        maskit(s, 1);
        const float m = swap32_max(rowmax(s));
        setm(m);
#pragma unroll
        for (int r = 0; r < 16; r++) { o[r] = 0.f; s[r] -= m; }
        pv(s, qt + 8);
      }
      TSB(18);
      TSB(19);
#pragma unroll 1
      for (int G = qt + 7; G >= 8; G--) { f32x16 s = qk(G); pv(s, G); }
      TSB(4);
      if (HELPER) { wait_vm<0>(); hk(); }
      TSB(5);
      bar();
      if (!HELPER && w < 2) wissue<20480, DstO, 0, 4>(ws + WS_WO + ctB * 102400, w, l);
      if (!HELPER && layer == 0) {
        fill_holes<2, true, 0>(rs, WS_O + 0x280000 + ((4 * rbA + w) * 80 + 5 * ctA) * 512, l);
        if (w >= 2) fill_holes<5, false, 0>(rs, WS_X2 + 0x280000 + ((rbB * 2 + (w - 2)) * 80 + ctB * 10) * 512, l);
        if (w == 0) fill_holes<0, true, 0>(rs, WS_ST + 2 * 131072 + (rbB * 8 + ctB) * 512, l);
      }
      TSB(6);
      {
        const int glo = gmin > qt + 1 ? gmin : qt + 1;
#pragma unroll 1
        for (int G = 7; G >= glo; G--) { f32x16 s = qk(G); pv(s, G); }
        if (gmin == 0) { f32x16 s = qk(qt); maskit(s, 2); pv(s, qt); }
      }
      float lsum;
      { auto r = __builtin_amdgcn_permlane32_swap(__float_as_uint(o[8]), __float_as_uint(o[8]), false, false); lsum = __uint_as_float(r[1]); }
      if (__builtin_amdgcn_ballot_w64(!(lsum <= 1.0995116e12f && lsum >= 0.25f)) != 0) {
        q1u[2] = 0u; q1u[3] = 0u;
        const int g0 = gmin > qt ? gmin : qt;
        float m = NEG;
#pragma unroll 1
        for (int G = g0; G <= qt + 8; G++) { f32x16 s = qk(G); maskit(s, G == qt + 8 ? 1 : (G == qt ? 2 : 0)); m = fmaxf(m, rowmax(s)); }
        m = swap32_max(m);
#pragma unroll
        for (int r = 0; r < 16; r++) o[r] = 0.f;
#pragma unroll 1
        for (int G = g0; G <= qt + 8; G++) {
          f32x16 s = qk(G);
          maskit(s, G == qt + 8 ? 1 : (G == qt ? 2 : 0));
#pragma unroll
          for (int r = 0; r < 16; r++) s[r] -= m;
          pv(s, G);
        }
        auto r = __builtin_amdgcn_permlane32_swap(__float_as_uint(o[8]), __float_as_uint(o[8]), false, false);
        lsum = __uint_as_float(r[1]);
      }
      const float inv = 1.0f / lsum;
      unsigned pk[6];
#pragma unroll
      for (int r = 0; r < 6; r++) pk[r] = fillw(cvtpk(o[2 * r] * inv, o[2 * r + 1] * inv));
      sw32(pk[0], pk[2]); sw32(pk[1], pk[3]);
      const unsigned oa = lb + L_Q + qt * 2560 + a31 * 16;
      *(LAS u32x4*)(oa + (2 * hd + h) * 512) = (u32x4){pk[0], pk[1], pk[2], pk[3]};
      if (h == 0) *(LAS u32x2*)(oa + 2048 + hd * 8) = (u32x2){pk[4], pk[5]};
      TSB(7);
      if (!HELPER) wait_vm<0>();
      wait_lgkm_bar();
      if (!HELPER) {
        const unsigned so = WS_O + layer * 0x280000 + ((4 * rbA + w) * 80 + 5 * ctA) * 512;
        const unsigned la = lb + L_Q + w * 2560 + l * 16;
        const u32x4 v0 = lds_rdu4(la), v1 = lds_rdu4(la + 1024);
        st16(rs, l * 16, so, v0);
        st16(rs, l * 16 + 1024, so, v1);
        if (l < 32) st16(rs, l * 16 + 2048, so, lds_rdu4(la + 2048));
      } else if (w >= 6) {
        wissue<20480, DstO, 4, 5>(ws + WS_WO + ctB * 102400, w - 6, l);
      }
      TSB(8);
  };
  auto res_init = [&](f32x4(&acc0)[5], f32x4(&acc1)[5], bool bias, int l) {
    const int g = l >> 4;
#pragma unroll
    for (int m = 0; m < 5; m++) {
      acc0[m] = (f32x4){bflo(res[m][0]), bfhi(res[m][0]), bflo(res[m][1]), bfhi(res[m][1])};
      if (bias) acc0[m] += lds_rdf4(lb + L_B2 + (m * 16 + g * 4) * 4);
      acc1[m] = (f32x4){0.f, 0.f, 0.f, 0.f};
    }
  };
  auto res_epilogue = [&](f32x4(&acc0)[5], unsigned ex, bool last, unsigned xdst, int stbuf, int l, int w, int rbB, int ctB) {
    const int g = l >> 4, i = l & 15;
    const unsigned exr = lb + ex + (w ^ 2) * 5120 + l * 16;
#pragma unroll
    for (int m = 0; m < 5; m++) {
      const f32x4 y = acc0[m] + lds_rdf4(exr + m * 1024);
      res[m] = (u32x2){fillw(cvtpk(y[0], y[1])), fillw(cvtpk(y[2], y[3]))};
    }
    if (!last) {
      if (RESB) store_res(xdst, l, w, rbB, ctB);
      wait_lgkm_bar();
      if (!RESB) store_res(xdst, l, w, rbB, ctB);
      put_stats(res, stbuf, l, w, rbB, ctB);
    } else {
      const int tok = rbB * 64 + (w & 1) * 32 + (w >> 1) * 16 + i, ch = (g & 1) * 2 + (g >> 1);
      u32x4 c[3];
      res_chunks(c);
      char* op = (char*)out + (size_t)tok * 1280 + ctB * 160;
      *(u32x4*)(op + ch * 16) = c[0];
      *(u32x4*)(op + (4 + ch) * 16) = c[1];
      if ((g & 1) == 0) *(u32x4*)(op + (8 + (g >> 1)) * 16) = c[2];
    }
  };
  if (w0 < 4) {
#pragma unroll 1
    for (int layer = 0; layer < 2; layer++) {
      [[maybe_unused]] const int tb = 4 + layer * 24;
      SETP(0);
      {
      LANE_VARS
      f32x16 acc[4];
#pragma unroll
      for (int a = 0; a < 4; a++)
#pragma unroll
        for (int r = 0; r < 16; r++) acc[a][r] = 0.f;
      unsigned tms = lb + L_MS + (w * 32 + a31) * 8, tcs = lb + L_CSQ + h * 16;
      unsigned wb0 = lb + 65536 + h * 1920 + a31 * 16, wb1 = wb0 + 61440, wb2 = wb0 - 65536;
      keep(tms); keep(tcs); keep(wb0); keep(wb1); keep(wb2);
      f32x2 ms;
      f32x4 cs[4][4];
      gemm32x<0, 4, XRQ, 22, true, 5, WINQ>(acc, ws + WS_X2 + layer * 0x280000 + (size_t)(4 * rbA + w) * 80 * 512, l * 16, layer == 0, [&](auto k) {
        constexpr int st = k / 4, o = (k & 3) * 3840;
        if constexpr (st < 4) return wb0 + (st * 15360 + o);
        else if constexpr (st < 6) return wb1 + ((st - 4) * 15360 + o);
        else return wb2 + ((st - 6) * 15360 + o);
      }, [&](auto r) {
        if constexpr (r == 0) ms = lds_rdf2(tms);
        else cs[3][r - 1] = lds_rdf4(tcs + 3 * 128 + (r - 1) * 32);
      } FPASS(0));
      ms_ready(ms, tms);
      TSB(1);
      SETP(1);
      const unsigned kvs = WS_KV + (layer * 16 + ctA) * 2 * 163840 + (4 * rbA + w) * 2560 - 163840;
      const float mr = -ms[0], rk = ms[1], rq = ms[1] * QSCALE;
      const unsigned pk_ = a31 * 16, pv4 = (a31 ^ 4) * 16, pv8 = (a31 ^ 8) * 16;
      const unsigned lt = lb + w * 2560;
      static_for<0, 4>([&](auto aa) {
        constexpr int a = 3 - aa;
        if constexpr (a > 0) static_for<0, 4>([&](auto q) { cs[a - 1][q] = lds_rdf4(tcs + (a - 1) * 128 + q * 32); });
        sbar();
        unsigned pk[8];
        static_for<0, 4>([&](auto q) {
          const float r1 = (a == 0 || (a == 1 && q == 0)) ? rq : rk;
          f32x2 v0 = {acc[a][4 * q + 0], acc[a][4 * q + 1]}, v1 = {acc[a][4 * q + 2], acc[a][4 * q + 3]};
          v0 = (v0 + (f32x2){cs[a][q][0], cs[a][q][1]} * mr) * r1;
          v1 = (v1 + (f32x2){cs[a][q][2], cs[a][q][3]} * mr) * r1;
          pk[2 * q] = cvtpk(v0[0], v0[1]); pk[2 * q + 1] = cvtpk(v1[0], v1[1]);
        });
        static_for<0, 2>([&](auto pr) {
          sw32(pk[4 * pr], pk[4 * pr + 2]); sw32(pk[4 * pr + 1], pk[4 * pr + 3]);
          const u32x4 v = {pk[4 * pr], pk[4 * pr + 1], pk[4 * pr + 2], pk[4 * pr + 3]};
          auto off = [&](auto ci_) {
            constexpr int ci = ci_, which = ci / 5, c = ci % 5;
            return (which == 2 ? (c == 4 ? pv8 : ((c & 1) ? pv4 : pk_)) : pk_) + c * 512;
          };
          constexpr int c0 = 4 * a + 2 * pr, c1 = c0 + 1, w0_ = c0 / 5, w1_ = c1 / 5;
          const unsigned o0 = off(IC<c0>{}), o1 = off(IC<c1>{});
          constexpr unsigned l0_ = w0_ == 0 ? L_Q : (w0_ == 1 ? L_K + 20480 : L_V + 20480), l1_ = w1_ == 0 ? L_Q : (w1_ == 1 ? L_K + 20480 : L_V + 20480);
          if constexpr (w0_ >= 1 || w1_ >= 1) {
            const unsigned g0 = w0_ >= 1 ? o0 + w0_ * 163840 : 0x80000000u, g1 = (w1_ == 1 || w1_ == 2) ? o1 + w1_ * 163840 : 0x80000000u;
            st16(rs, h ? g1 : g0, kvs, v);
          }
          if constexpr (w1_ == 3) { if (h == 0) *(LAS u32x4*)(lt + l0_ + o0) = v; }
          else *(LAS u32x4*)(lt + (h ? l1_ + o1 : l0_ + o0)) = v;
        });
        sbar();
      });
      wait_lgkm_bar();
      }
      SETP(2);
      attention(std::false_type{}, layer, [] {});
      SETP(3);
      {
        LANE_VARS
        const int wt = w & 1, wk = w >> 1;
        f32x4 acc0[5], acc1[5];
        res_init(acc0, acc1, false, l);
        unsigned wb0 = lb + 73728 + wk * 10240 + g * 1280 + i * 16, wb1 = wb0 + 61440, wb2 = wb0 - 73728;
        keep(wb0); keep(wb1); keep(wb2);
        const unsigned xv = (8 * wk + g) * 512 + i * 16;
        gemm16x<200, 5, 10, false, WINO>(acc0, acc1, ws + WS_O + layer * 0x280000 + (size_t)(2 * rbB + wt) * 80 * 512, xv + wk * 256, xv + (1 - wk) * 256, [&](auto k) {
          constexpr int st = k / 2, o = (k & 1) * 5120;
          if constexpr (st < 3) return wb0 + (st * 20480 + o);
          else if constexpr (st == 3) return wb1 + o;
          else return wb2 + o;
        }, lb + L_EX + w * 5120 + l * 16 FPASS(1));
        TSB(10);
        res_epilogue(acc0, L_EX, false, WS_X1 + layer * 0x280000, 2 * layer + 1, l, w, rbB, ctB);
        TSB(11);
      }
      SETP(4);
      {
        LANE_VARS
        f32x16 acc[3];
#pragma unroll
        for (int a = 0; a < 3; a++)
#pragma unroll
          for (int r = 0; r < 16; r++) acc[a][r] = 0.f;
        unsigned tms = lb + L_MS + (w * 32 + a31) * 8, tcs = lb + L_CS1 + h * 16;
        unsigned wb0 = lb + h * 1536 + a31 * 16, wb1 = wb0 + 61440;
        keep(tms); keep(tcs); keep(wb0); keep(wb1);
        f32x2 ms;
        f32x4 cs[3][4], bb[3][4];
        gemm32x<100, 3, XR1, DEF1 ? 16 : -1, false, 9, WIN1>(acc, ws + WS_X1 + layer * 0x280000 + (size_t)(4 * rbA + w) * 80 * 512, l * 16, DEF1 != 0, [&](auto k) {
          if constexpr (k < 20) return wb0 + k * 3072; else return wb1 + (k - 20) * 3072;
        }, [&](auto r) {
          if constexpr (r == 0) ms = lds_rdf2(tms);
          else if constexpr (r < 5) cs[0][r - 1] = lds_rdf4(tcs + (r - 1) * 32);
          else bb[0][r - 5] = lds_rdf4(tcs + 512 + (r - 5) * 32);
        } FPASS(2));
        ms_ready(ms, tms);
        wait_lgkm_bar();
        TSB(13);
        const float rstd = ms[1], mr = -ms[0] * ms[1];
        const unsigned so = WS_H + layer * 0x600000 + ((4 * rbA + w) * 192 + (HPERM ? 4 : 12) * ctA) * 512;
#pragma unroll
        for (int a = 0; a < 3; a++) {
          if (a < 2) {
#pragma unroll
            for (int q = 0; q < 4; q++) {
              cs[a + 1][q] = lds_rdf4(tcs + (a + 1) * 128 + q * 32);
              bb[a + 1][q] = lds_rdf4(tcs + 512 + (a + 1) * 128 + q * 32);
            }
          }
          sbar();
          unsigned pk[8];
#pragma unroll
          for (int q = 0; q < 4; q++) {
            const f32x4 c4 = cs[a][q], b4 = bb[a][q];
            f32x2 t0 = (f32x2){c4[0], c4[1]} * mr + (f32x2){b4[0], b4[1]}, t1 = (f32x2){c4[2], c4[3]} * mr + (f32x2){b4[2], b4[3]};
            f32x2 v0 = (f32x2){acc[a][4 * q + 0], acc[a][4 * q + 1]} * rstd + t0, v1 = (f32x2){acc[a][4 * q + 2], acc[a][4 * q + 3]} * rstd + t1;
            v0 = (f32x2){fmaxf(v0[0], 0.f), fmaxf(v0[1], 0.f)}; v1 = (f32x2){fmaxf(v1[0], 0.f), fmaxf(v1[1], 0.f)};
            v0 *= v0; v1 *= v1;
            pk[2 * q] = fillw(cvtpk(v0[0], v0[1])); pk[2 * q + 1] = fillw(cvtpk(v1[0], v1[1]));
          }
#pragma unroll
          for (int pr = 0; pr < 2; pr++) {
            sw32(pk[4 * pr], pk[4 * pr + 2]); sw32(pk[4 * pr + 1], pk[4 * pr + 3]);
            st16(rs, (2 * pr + h) * 512 + a31 * 16, so + a * (HPERM ? 64 : 4) * 512, (u32x4){pk[4 * pr], pk[4 * pr + 1], pk[4 * pr + 2], pk[4 * pr + 3]});
          }
          sbar();
        }
        TSB(14);
      }
      SETP(5);
      {
        LANE_VARS
        const int wt = w & 1, wk = w >> 1;
        f32x4 acc0[5], acc1[5];
        res_init(acc0, acc1, true, l);
        unsigned wb0 = lb + wk * 10240 + g * 1280 + i * 16, wb1 = wb0 + 61440, wb2 = wb0 + 122880;
        keep(wb0); keep(wb1); keep(wb2);
        const unsigned xv = (8 * wk + g) * 512 + i * 16;
        gemm16x<300, 12, XR2, true, WIN2>(acc0, acc1, ws + WS_H + layer * 0x600000 + (size_t)(2 * rbB + wt) * 192 * 512, xv + wk * 256, xv + (1 - wk) * 256, [&](auto k) {
          constexpr int sl7 = (k / 2) % 7, o = (k & 1) * 5120;
          if constexpr (sl7 < 3) return wb0 + (sl7 * 20480 + o);
          else if constexpr (sl7 < 6) return wb1 + ((sl7 - 3) * 20480 + o);
          else return wb2 + o;
        }, lb + L_EX2 + w * 5120 + l * 16 FPASS(3));
        TSB(16);
        res_epilogue(acc0, L_EX2, layer == 1, WS_X2 + 0x280000, 2, l, w, rbB, ctB);
      }
      TSB(17);
    }
  } else {
#pragma unroll 1
    for (int layer = 0; layer < 2; layer++) {
      [[maybe_unused]] const int tb = 4 + layer * 24;
      u32x4 parkA[30], parkB[25];
      {
      LANE_VARS
      const int p = w - 4;
      if (layer == 0 && w >= 6) wait_vm<(PST ? 32 : 0)>();
      TSB(0);
      bar();
      if (layer == 0) {
        if (w == 4) {
          u32x2 pc, cq[16];
          gld8<0>(pc, (l & 15) * 8, ws + WS_P1C + ctA * 128);
          stats_token(ws + WS_ST + 2 * rbA * 4096, l, lb + L_MS + l * 8);
          wait_vm<0>();
          keep(pc);
          if (__builtin_amdgcn_ballot_w64(pc[0] != (unsigned)E || pc[1] != (unsigned)(E >> 32)) != 0) poll(ws, WS_P1C + (ctA * 16 + (l & 15)) * 8, l < 16, E);
          static_for<0, 16>([&](auto r) { gld8<(r & 7) * 512>(cq[r], (l < 60 ? l : 59) * 8, ws + WS_CSQ + (ctA * 16 + (r >> 3) * 8) * 512); });
          if (PST) bar();
          wait_vm<0>();
          static_for<0, 16>([&](auto r) { keep(cq[r]); });
          f32x2 s = {0.f, 0.f};
#pragma unroll
          for (int r = 0; r < 16; r++) s += (f32x2){__uint_as_float(cq[r][0]), __uint_as_float(cq[r][1])};
          if (l < 60) *(LAS f32x2*)(lb + L_CSQ + l * 8) = s;
          if (PST) bar();
        } else if (w == 5) {
          stats_token(ws + WS_ST + (2 * rbA + 1) * 4096, l, lb + L_MS + (64 + l) * 8);
          if (PST) { bar(); bar(); }
        } else {
          if (PST) { wait_vm<16>(); bar(); } else __builtin_amdgcn_s_sleep(WDELAY);
          wissue<15360, DstQ, 6, 10>(ws + WS_WQ + ctA * 153600, w - 6, l);
          if (PST) { wait_vm<32>(); bar(); }
          wait_vm<0>();
        }
      } else if (w < 6) {
        stats_token(ws + WS_ST + (2 * layer) * 131072 + (2 * rbA + p) * 4096, l, lb + L_MS + (p * 64 + l) * 8);
      }
      if (layer == 0) {
        u32x4 pf[2];
        gld16<0>(pf[0], l * 32, ws + WS_P2); gld16<16>(pf[1], l * 32, ws + WS_P2);
        wait_lgkm_bar();
        wait_vm<0>();
        keep(pf[0]); keep(pf[1]);
        const unsigned e0 = (unsigned)E, e1 = (unsigned)(E >> 32);
        const bool ok = pf[0][0] == e0 && pf[0][1] == e1 && pf[0][2] == e0 && pf[0][3] == e1 && pf[1][0] == e0 && pf[1][1] == e1 && pf[1][2] == e0 && pf[1][3] == e1;
        if (__builtin_amdgcn_ballot_w64(!ok) != 0)
          for (int r = 0; r < 4; r++) poll(ws, WS_P2 + (r * 64 + l) * 8, true, E);
        if (bid == 0 && w == 6 && l == 0) st_flag(ws, 0, E);
      }
      {
      static_for<0, 3>([&](auto a) { fill_holes<2, false, FSLP>(rs, WS_H + layer * 0x600000 + ((4 * rbA + p) * 192 + 4 * ctA + 64 * a) * 512, l); });
      if (p < 2) fill_holes<5, false, FSLP>(rs, WS_X1 + layer * 0x280000 + ((rbB * 2 + p) * 80 + ctB * 10) * 512, l);
      if (p == 2) fill_holes<0, true, 0>(rs, WS_ST + (2 * layer + 1) * 131072 + (rbB * 8 + ctB) * 512, l);
      }
      const char* gw1 = ws + WS_W1 + ctA * 122880 + p * 30720;
      wait_vm<0>();
      bar();
      bar();
      park_load<0, 30, LSLPA>(parkA, gw1, l);
      }
      attention(std::true_type{}, layer, [&] { park_keep(parkA); });
      LANE_VARS
      const int p = w - 4;
      const char* gw2 = ws + WS_W2 + ctB * 245760;
      {
        if (layer == 0 && w == 7) {
          u32x2 c1[16];
          static_for<0, 16>([&](auto r) { gld8<(r & 7) * 512>(c1[r], (l < 48 ? l : 47) * 8, ws + WS_CS1 + (ctA * 16 + (r >> 3) * 8) * 512); });
          wait_vm<0>();
          static_for<0, 16>([&](auto r) { keep(c1[r]); });
          f32x2 s = {0.f, 0.f};
#pragma unroll
          for (int r = 0; r < 16; r++) s += (f32x2){__uint_as_float(c1[r][0]), __uint_as_float(c1[r][1])};
          if (l < 48) *(LAS f32x2*)(lb + L_CS1 + l * 8) = s;
        }
      }
      if (!WB4) wait_vm<0>();
      TSB(9);
      bar();
      {
        const char* gp = gw2 + 7 * 20480 + p * 5120 + 2048;
        opaque_s(gp);
        const unsigned v0 = l * 16;
        static_for<0, 25>([&](auto j) {
          gld16<(j % 5) * 1024 - 2048>(parkB[j], v0, gp);
          if constexpr (j % 5 == 4) { gp += 20480; opaque_s(gp); }
          if constexpr (LSLPB > 0) __builtin_amdgcn_s_sleep(LSLPB);
        });
      }
      if (WB4) { wait_vm<25>(); bar(); }
      bar();
      bar();
      unsigned pd = lb + p * 30720 + l * 16;
      keep(pd);
      if (!DEF1 || w < 6) park_put(parkA, pd);
      wait_lgkm();
      TSB(11);
      bar();
      if (DEF1 && w >= 6) { park_put(parkA, pd); wait_lgkm(); }
      if (w < 6) stats_token(ws + WS_ST + (2 * layer + 1) * 131072 + (2 * rbA + p) * 4096, l, lb + L_MS + (p * 64 + l) * 8);
      else wissue<20480, Dst2, 6, 7>(gw2, w - 6, l);
      TSB(12);
      if (DEF1) {
        park_load<0, DEF1, LSLP>(parkA, gw2 + p * 30720, l);
        bar();
        park_load<DEF1, 30, LSLP>(parkA, gw2 + p * 30720, l);
      } else {
        park_load<0, 30, LSLP>(parkA, gw2 + p * 30720, l);
      }
      wait_lgkm_bar();
      wait_vm<0>();
      park_keep(parkA);
      park_keep(parkB);
      if (!DEFP || w < 6) park_put(parkA, pd);
      if (w < 6) *(LAS u32x2*)(lb + L_MS + (p * 64 + l) * 8) = (u32x2){HOLE, HOLE};
      wait_lgkm();
      TSB(14);
      bar();
      if (DEFP && w >= 6) { park_put(parkA, pd); wait_lgkm(); }
      const char* gwq = ws + WS_WQ + ctA * 153600 + p * 30720;
      unsigned pb = lb + p * 5120 + l * 16;
      keep(pb);
      auto put = [&](auto J0, auto J1) {
        static_for<J0, J1>([&](auto j) {
          *(LAS u32x4*)(pb + (Dst2::at(7 + j / 5) + (j % 5) * 1024)) = parkB[j];
          if constexpr (j + 1 < J1) __builtin_amdgcn_s_sleep(PSLP);
        });
        wait_lgkm();
      };
      if (layer == 0) park_load<0, 6, 0>(parkA, gwq, l);
      bar();
      if (layer == 0) park_load<6, 12, 0>(parkA, gwq, l);
      put(IC<0>{}, IC<7>{});
      bar();
      if (layer == 0) park_load<12, 18, 0>(parkA, gwq, l);
      put(IC<7>{}, IC<13>{});
      bar();
      if (layer == 0) park_load<18, 24, 0>(parkA, gwq, l);
      put(IC<13>{}, IC<20>{});
      bar();
      if (layer == 0) park_load<24, 30, 0>(parkA, gwq, l);
      put(IC<20>{}, IC<25>{});
      bar();
      bar();
      if (layer == 0) {
        wait_vm<0>();
        park_keep(parkA);
        bar();
        if (w >= 6) wissue<15360, DstQ, 8, 10>(ws + WS_WQ + ctA * 153600, w - 6, l);
        unsigned pq = lb + (p < 3 ? 65536 + p * 30720 : 0) + l * 16;
        keep(pq);
        park_put(parkA, pq);
        wait_lgkm();
        wait_vm<0>();
      }
      TSB(17);
    }
  }
}

extern "C" void kernel_launch(void* const* d_in, const int* in_sizes, int n_in, void* d_out, int out_size, void* d_ws, size_t ws_size,
                              hipStream_t stream) {
  hipFuncSetAttribute((const void*)k_all, hipFuncAttributeMaxDynamicSharedMemorySize, LDS_BYTES);
  k_all<<<256, 512, LDS_BYTES, stream>>>((const u16*)d_in[0], (const u16*)d_in[1], (const u16*)d_in[2], (const u16*)d_in[4], (const u16*)d_in[5],
                                         (const u16*)d_in[6], (const u16*)d_in[7], (u16*)d_out, (char*)d_ws);
}
```
